```python
import torch

def _softmax(x, axis=-1):
    return torch.softmax(x, dim=axis)
def _gelu(x):
    return torch.nn.functional.gelu(x, approximate="tanh")
def _silu(x):
    return torch.nn.functional.silu(x)
def _sigmoid(x):
    return torch.sigmoid(x)
def _layernorm(x, axis=-1, eps=1e-5):
    m = x.mean(dim=axis, keepdim=True)
    v = x.var(dim=axis, keepdim=True, unbiased=False)
    return (x - m) / torch.sqrt(v + eps)
def _scatter_add(tgt, idx, src, axis=0):
    return tgt.clone().index_add_(axis, idx, src)
def _segment_sum(src, seg, n):
    out = torch.zeros((n,) + src.shape[1:], dtype=src.dtype)
    return out.index_add_(0, seg, src)
def _pad(x, pw):
    flat = tuple(v for pair in reversed(pw) for v in pair)
    return torch.nn.functional.pad(x, flat)

kernel_name = 'standalone__gqa_softmax__decode'

def setup_inputs():

    _w = (lambda a: (lambda t: t.to(torch.bfloat16) if t.is_floating_point() else t)(torch.as_tensor(a)))
    B, Hq, Hkv, Sq, Sk, D = 8, 32, 8, 1, 8192, 128
    return {
        "q": _w((lambda *s: torch.randn(*s, dtype=torch.bfloat16) * 0.8)(B, Hq, Sq, D)),
        "k": _w((lambda *s: torch.randn(*s, dtype=torch.bfloat16) * 0.8)(B, Hkv, Sk, D)),
        "v": _w((lambda *s: torch.randn(*s, dtype=torch.bfloat16) * 0.05)(B, Hkv, Sk, D)),
    }

def reference(q, k, v):
    B, Hq, Sq, D = q.shape
    Hkv = k.shape[1]
    rep = Hq // Hkv
    k = (lambda x, n, axis: torch.repeat_interleave(x, n, dim=axis))(k, rep, axis=1)
    v = (lambda x, n, axis: torch.repeat_interleave(x, n, dim=axis))(v, rep, axis=1)
    scale = D ** -0.5
    scores = torch.einsum("bhqd,bhkd->bhqk", q, k) * scale
    p = _softmax(scores, axis=-1)
    return torch.einsum("bhqk,bhkd->bhqd", p, v)
```

```cpp
#include <hip/hip_runtime.h>
#include <hip/hip_bf16.h>

typedef unsigned u32x4 __attribute__((ext_vector_type(4)));
typedef unsigned u32x2 __attribute__((ext_vector_type(2)));
typedef float f32x4 __attribute__((ext_vector_type(4)));
typedef float f32x2 __attribute__((ext_vector_type(2)));
typedef __bf16 bf16x8 __attribute__((ext_vector_type(8)));
typedef __bf16 bf16x2 __attribute__((ext_vector_type(2)));
typedef short s16x4 __attribute__((ext_vector_type(4)));

#define NSLOT 4
constexpr int SK = 8192, DH = 128, NPAIR = 64, GRP = 4, NW = 4;
constexpr int TILE = 16;
constexpr int NIT = SK / TILE / (GRP * NW);
constexpr unsigned PAIR_BYTES = SK * DH * 2;
constexpr unsigned TILE_BYTES = TILE * DH * 2;
constexpr unsigned SLOT_BYTES = 2 * TILE_BYTES;
constexpr unsigned RING_BYTES = NSLOT * SLOT_BYTES;
constexpr unsigned RED_OFF = NW * RING_BYTES;
constexpr unsigned RED_REC = (DH + 4) * 4;
constexpr unsigned LDS_BYTES = RED_OFF + NW * 4 * RED_REC;
constexpr float C2 = 0.088388347648318440f * 1.4426950408889634f;

__device__ __forceinline__ unsigned cvtpk(float lo, float hi) {
  bf16x2 v = {(__bf16)lo, (__bf16)hi};
  return __builtin_bit_cast(unsigned, v);
}
__device__ __forceinline__ float xg_max(float x) {
  auto s = __builtin_amdgcn_permlane16_swap(__float_as_uint(x), __float_as_uint(x), false, false);
  x = fmaxf(__uint_as_float(s[0]), __uint_as_float(s[1]));
  auto t = __builtin_amdgcn_permlane32_swap(__float_as_uint(x), __float_as_uint(x), false, false);
  return fmaxf(__uint_as_float(t[0]), __uint_as_float(t[1]));
}
__device__ __forceinline__ float xg_sum(float x) {
  auto s = __builtin_amdgcn_permlane16_swap(__float_as_uint(x), __float_as_uint(x), false, false);
  x = __uint_as_float(s[0]) + __uint_as_float(s[1]);
  auto t = __builtin_amdgcn_permlane32_swap(__float_as_uint(x), __float_as_uint(x), false, false);
  return __uint_as_float(t[0]) + __uint_as_float(t[1]);
}

#define DMA_TILE(m0k, soff)                                                                     \
  asm volatile("s_mov_b32 m0, %0\n\ts_nop 0\n\t"                                                \
               "buffer_load_dwordx4 %3, %1, %9 offen nt lds\n\t"                                \
               "buffer_load_dwordx4 %4, %1, %9 offen offset:1024 nt lds\n\t"                    \
               "buffer_load_dwordx4 %5, %1, %9 offen offset:2048 nt lds\n\t"                    \
               "buffer_load_dwordx4 %6, %1, %9 offen offset:3072 nt lds\n\t"                    \
               "s_add_u32 m0, m0, 0x1000\n\ts_nop 0\n\t"                                        \
               "buffer_load_dwordx4 %7, %2, %9 offen nt lds\n\t"                                \
               "buffer_load_dwordx4 %8, %2, %9 offen offset:1024 nt lds\n\t"                    \
               "buffer_load_dwordx4 %7, %2, %9 offen offset:2048 nt lds\n\t"                    \
               "buffer_load_dwordx4 %8, %2, %9 offen offset:3072 nt lds"                        \
               :: "s"(m0k), "s"(rk), "s"(rv), "v"(ks0), "v"(ks1), "v"(ks2), "v"(ks3), "v"(vs0), "v"(vs1), "s"(soff) \
               : "memory", "scc")

__global__ void __launch_bounds__(256, 2) gqa_split(const char* __restrict__ K, const char* __restrict__ V, const char* __restrict__ Q, float* __restrict__ part) {
  extern __shared__ __attribute__((aligned(16))) char lds[];
  const unsigned bid = blockIdx.x, pair = bid >> 2, j = bid & 3;
  const unsigned lane = threadIdx.x & 63, w = __builtin_amdgcn_readfirstlane(threadIdx.x >> 6);
  const unsigned g = lane >> 4, c = lane & 15;
  const __amdgpu_buffer_rsrc_t rk = __builtin_amdgcn_make_buffer_rsrc((void*)(K + (size_t)pair * PAIR_BYTES), 0, PAIR_BYTES, 0x00020000);
  const __amdgpu_buffer_rsrc_t rv = __builtin_amdgcn_make_buffer_rsrc((void*)(V + (size_t)pair * PAIR_BYTES), 0, PAIR_BYTES, 0x00020000);
  const unsigned ks0 = g * 256 + ((c ^ g) << 4), ks1 = ks0 ^ 64, ks2 = ks0 ^ 128, ks3 = ks0 ^ 192;
  const unsigned vs0 = g * 256 + ((c ^ (g << 2)) << 4), vs1 = vs0 ^ 32;
  const unsigned ldsb = (unsigned)(size_t)lds;
  const unsigned ring = ldsb + w * RING_BYTES;
  unsigned soff = (j * NW + w) * TILE_BYTES;
  u32x4 q0, q1, q2, q3;
  {
    const char* qp = Q + (size_t)pair * 1024;
    const unsigned qo = (c & 3) * 256 + g * 16;
    asm volatile("global_load_dwordx4 %0, %4, %5\n\tglobal_load_dwordx4 %1, %4, %5 offset:64\n\t"
                 "global_load_dwordx4 %2, %4, %5 offset:128\n\tglobal_load_dwordx4 %3, %4, %5 offset:192"
                 : "=&v"(q0), "=&v"(q1), "=&v"(q2), "=&v"(q3) : "v"(qo), "s"(qp) : "memory");
  }
#pragma unroll
  for (int s = 0; s < NSLOT; ++s) { const unsigned m0k = ring + s * SLOT_BYTES; DMA_TILE(m0k, soff); soff += GRP * NW * TILE_BYTES; }
  asm volatile("s_waitcnt vmcnt(%4)" : "+v"(q0), "+v"(q1), "+v"(q2), "+v"(q3) : "i"(8 * NSLOT) : "memory");

  const unsigned ka = c * 256 + ((g ^ c) << 4);
  const unsigned qq = (lane >> 2) & 3, pp = lane & 3;
  const unsigned va = TILE_BYTES + (4 * g + qq) * 256 + (((pp >> 1) ^ (qq << 2) ^ ((g & 1) << 1)) << 4) + 8 * (pp & 1);
  float m = -INFINITY, l = 0.f;
  f32x4 acc[8];
#pragma unroll
  for (int b = 0; b < 8; ++b) acc[b] = f32x4{0.f, 0.f, 0.f, 0.f};
  unsigned slot = ring;
#pragma unroll 1
  for (int it = 0; it < NIT; ++it) {
    u32x4 k0, k1, k2, k3; u32x2 v0, v1, v2, v3, v4, v5, v6, v7;
    const unsigned a = slot + ka, b0 = slot + va;
    asm volatile("s_waitcnt vmcnt(%24)\n\t"
                 "ds_read_b128 %0, %12\n\tds_read_b128 %1, %13\n\tds_read_b128 %2, %14\n\tds_read_b128 %3, %15\n\t"
                 "ds_read_b64_tr_b16 %4, %16\n\tds_read_b64_tr_b16 %5, %17\n\tds_read_b64_tr_b16 %6, %18\n\tds_read_b64_tr_b16 %7, %19\n\t"
                 "ds_read_b64_tr_b16 %8, %20\n\tds_read_b64_tr_b16 %9, %21\n\tds_read_b64_tr_b16 %10, %22\n\tds_read_b64_tr_b16 %11, %23\n\t"
                 "s_waitcnt lgkmcnt(0)"
                 : "=&v"(k0), "=&v"(k1), "=&v"(k2), "=&v"(k3), "=&v"(v0), "=&v"(v1), "=&v"(v2), "=&v"(v3), "=&v"(v4), "=&v"(v5), "=&v"(v6), "=&v"(v7)
                 : "v"(a), "v"(a ^ 64), "v"(a ^ 128), "v"(a ^ 192),
                   "v"(b0), "v"(b0 ^ 32), "v"(b0 ^ 64), "v"(b0 ^ 96), "v"(b0 ^ 128), "v"(b0 ^ 160), "v"(b0 ^ 192), "v"(b0 ^ 224),
                   "i"(8 * (NSLOT - 1))
                 : "memory");
    DMA_TILE(slot, soff);
    soff += GRP * NW * TILE_BYTES;
    slot += SLOT_BYTES; if (slot == ring + RING_BYTES) slot = ring;
    f32x4 s = {0.f, 0.f, 0.f, 0.f};
    s = __builtin_amdgcn_mfma_f32_16x16x32_bf16(__builtin_bit_cast(bf16x8, k0), __builtin_bit_cast(bf16x8, q0), s, 0, 0, 0);
    s = __builtin_amdgcn_mfma_f32_16x16x32_bf16(__builtin_bit_cast(bf16x8, k1), __builtin_bit_cast(bf16x8, q1), s, 0, 0, 0);
    s = __builtin_amdgcn_mfma_f32_16x16x32_bf16(__builtin_bit_cast(bf16x8, k2), __builtin_bit_cast(bf16x8, q2), s, 0, 0, 0);
    s = __builtin_amdgcn_mfma_f32_16x16x32_bf16(__builtin_bit_cast(bf16x8, k3), __builtin_bit_cast(bf16x8, q3), s, 0, 0, 0);
    const float tm = fmaxf(fmaxf(s[0], s[1]), fmaxf(s[2], s[3])) * C2;
    if (__builtin_amdgcn_ballot_w64(tm > m) != 0) {
      const float mn = fmaxf(m, xg_max(tm));
      const float al = __builtin_amdgcn_exp2f(m - mn);
      m = mn; l *= al;
#pragma unroll
      for (int b = 0; b < 8; ++b) acc[b] *= al;
    }
    const float p0 = __builtin_amdgcn_exp2f(fmaf(s[0], C2, -m)), p1 = __builtin_amdgcn_exp2f(fmaf(s[1], C2, -m));
    const float p2 = __builtin_amdgcn_exp2f(fmaf(s[2], C2, -m)), p3 = __builtin_amdgcn_exp2f(fmaf(s[3], C2, -m));
    l += (p0 + p1) + (p2 + p3);
    const u32x2 pk = {cvtpk(p0, p1), cvtpk(p2, p3)};
    const s16x4 pb = __builtin_bit_cast(s16x4, pk);
    acc[0] = __builtin_amdgcn_mfma_f32_16x16x16bf16_1k(__builtin_bit_cast(s16x4, v0), pb, acc[0], 0, 0, 0);
    acc[1] = __builtin_amdgcn_mfma_f32_16x16x16bf16_1k(__builtin_bit_cast(s16x4, v1), pb, acc[1], 0, 0, 0);
    acc[2] = __builtin_amdgcn_mfma_f32_16x16x16bf16_1k(__builtin_bit_cast(s16x4, v2), pb, acc[2], 0, 0, 0);
    acc[3] = __builtin_amdgcn_mfma_f32_16x16x16bf16_1k(__builtin_bit_cast(s16x4, v3), pb, acc[3], 0, 0, 0);
    acc[4] = __builtin_amdgcn_mfma_f32_16x16x16bf16_1k(__builtin_bit_cast(s16x4, v4), pb, acc[4], 0, 0, 0);
    acc[5] = __builtin_amdgcn_mfma_f32_16x16x16bf16_1k(__builtin_bit_cast(s16x4, v5), pb, acc[5], 0, 0, 0);
    acc[6] = __builtin_amdgcn_mfma_f32_16x16x16bf16_1k(__builtin_bit_cast(s16x4, v6), pb, acc[6], 0, 0, 0);
    acc[7] = __builtin_amdgcn_mfma_f32_16x16x16bf16_1k(__builtin_bit_cast(s16x4, v7), pb, acc[7], 0, 0, 0);
  }
  l = xg_sum(l);
  char* red = lds + RED_OFF;
  if (c < 4) {
    char* rec = red + (w * 4 + c) * RED_REC;
#pragma unroll
    for (int b = 0; b < 8; ++b) *(f32x4*)(rec + (16 * b + 4 * g) * 4) = acc[b];
    if (g == 0) *(f32x2*)(rec + DH * 4) = f32x2{m, l};
  }
  asm volatile("s_waitcnt lgkmcnt(0)" ::: "memory");
  __builtin_amdgcn_s_barrier();
  asm volatile("" ::: "memory");
  {
    f32x2 ml[NW], o[NW];
#pragma unroll
    for (int x = 0; x < NW; ++x) { const char* rec = red + (x * 4 + w) * RED_REC; ml[x] = *(const f32x2*)(rec + DH * 4); o[x] = *(const f32x2*)(rec + lane * 8); }
    const float M = fmaxf(fmaxf(ml[0][0], ml[1][0]), fmaxf(ml[2][0], ml[3][0]));
    float L = 0.f, o0 = 0.f, o1 = 0.f;
#pragma unroll
    for (int x = 0; x < NW; ++x) { const float a = __builtin_amdgcn_exp2f(ml[x][0] - M); L = fmaf(a, ml[x][1], L); o0 = fmaf(a, o[x][0], o0); o1 = fmaf(a, o[x][1], o1); }
    ((f32x4*)part)[((pair * 4 + w) * 4 + j) * 64 + lane] = f32x4{o0, o1, M, L};
  }
}

__global__ void __launch_bounds__(64) gqa_combine(const float* __restrict__ part, unsigned* __restrict__ out) {
  const unsigned row = blockIdx.x, lane = threadIdx.x;
  f32x4 r[GRP];
#pragma unroll
  for (int x = 0; x < GRP; ++x) r[x] = ((const f32x4*)part)[(row * 4 + x) * 64 + lane];
  const float M = fmaxf(fmaxf(r[0][2], r[1][2]), fmaxf(r[2][2], r[3][2]));
  float L = 0.f, o0 = 0.f, o1 = 0.f;
#pragma unroll
  for (int x = 0; x < GRP; ++x) { const float a = __builtin_amdgcn_exp2f(r[x][2] - M); L = fmaf(a, r[x][3], L); o0 = fmaf(a, r[x][0], o0); o1 = fmaf(a, r[x][1], o1); }
  const float inv = 1.f / L;
  out[row * 64 + lane] = cvtpk(o0 * inv, o1 * inv);
}

extern "C" void kernel_launch(void* const* d_in, const int* in_sizes, int n_in, void* d_out, int out_size, void* d_ws, size_t ws_size, hipStream_t stream) {
  (void)hipFuncSetAttribute((const void*)gqa_split, hipFuncAttributeMaxDynamicSharedMemorySize, LDS_BYTES);
  gqa_split<<<NPAIR * GRP, 256, LDS_BYTES, stream>>>((const char*)d_in[1], (const char*)d_in[2], (const char*)d_in[0], (float*)d_ws);
  gqa_combine<<<NPAIR * 4, 64, 0, stream>>>((const float*)d_ws, (unsigned*)d_out);
}
```

```cpp
#include <hip/hip_runtime.h>
#include <hip/hip_bf16.h>

typedef unsigned u32x4 __attribute__((ext_vector_type(4)));
typedef unsigned u32x2 __attribute__((ext_vector_type(2)));
typedef float f32x4 __attribute__((ext_vector_type(4)));
typedef float f32x2 __attribute__((ext_vector_type(2)));
typedef __bf16 bf16x8 __attribute__((ext_vector_type(8)));
typedef __bf16 bf16x2 __attribute__((ext_vector_type(2)));
typedef short s16x4 __attribute__((ext_vector_type(4)));
typedef __attribute__((address_space(1))) unsigned gu32;
typedef __attribute__((address_space(3))) unsigned lu32;
#define RLX_AGENT __ATOMIC_RELAXED, __HIP_MEMORY_SCOPE_AGENT

#define STAMP(i)
#define NSLOT 2
#define DYN 1
#define LAY 1
#define NSTAT 24
#define LEAD 1
constexpr int SK = 8192, DH = 128, NPAIR = 64, GRP = 4, NW = 4;
constexpr int TILE = 16;
constexpr int NUNIT = SK / (TILE * NW);
constexpr int S_IT = DYN ? NSTAT : NUNIT / GRP;
constexpr unsigned NDYN = NUNIT - S_IT * GRP;
static_assert(NDYN < 62, "ticket table is one LDS word per lane");
constexpr unsigned PAIR_BYTES = SK * DH * 2;
constexpr unsigned TILE_BYTES = TILE * DH * 2;
constexpr unsigned UNIT_BYTES = NW * TILE_BYTES;
constexpr unsigned SLOT_BYTES = 2 * TILE_BYTES;
constexpr unsigned RING_BYTES = NSLOT * SLOT_BYTES;
constexpr unsigned RED_OFF = NW * RING_BYTES;
constexpr unsigned RED_REC = (DH + 4) * 4;
constexpr unsigned TK_OFF = RED_OFF + NW * 4 * RED_REC;
constexpr unsigned MSK_OFF = TK_OFF + 256;
constexpr unsigned LDS_NEED = MSK_OFF + 1024;
constexpr unsigned LDS_BYTES = LDS_NEED > 81 * 1024 ? LDS_NEED : 81 * 1024;
constexpr float C2 = 0.088388347648318440f * 1.4426950408889634f;
constexpr unsigned G_WS = 32768, WS_CTL = 16384;
constexpr unsigned NOTREADY = 0xFFFFFFFFu;
constexpr unsigned SPIN_CAP = 1u << 20;

__device__ __forceinline__ unsigned cvtpk(float lo, float hi) {
  bf16x2 v = {(__bf16)lo, (__bf16)hi};
  return __builtin_bit_cast(unsigned, v);
}
__device__ __forceinline__ float xg_max(float x) {
  auto s = __builtin_amdgcn_permlane16_swap(__float_as_uint(x), __float_as_uint(x), false, false);
  x = fmaxf(__uint_as_float(s[0]), __uint_as_float(s[1]));
  auto t = __builtin_amdgcn_permlane32_swap(__float_as_uint(x), __float_as_uint(x), false, false);
  return fmaxf(__uint_as_float(t[0]), __uint_as_float(t[1]));
}
__device__ __forceinline__ float xg_sum(float x) {
  auto s = __builtin_amdgcn_permlane16_swap(__float_as_uint(x), __float_as_uint(x), false, false);
  x = __uint_as_float(s[0]) + __uint_as_float(s[1]);
  auto t = __builtin_amdgcn_permlane32_swap(__float_as_uint(x), __float_as_uint(x), false, false);
  return __uint_as_float(t[0]) + __uint_as_float(t[1]);
}
__device__ __forceinline__ unsigned lsb4(u32x4 x) { return (x[0] & 1) | ((x[1] & 1) << 1) | ((x[2] & 1) << 2) | ((x[3] & 1) << 3); }

#define DMA_TILE(m0k, soff)                                                                     \
  asm volatile("s_mov_b32 m0, %0\n\ts_nop 0\n\t"                                                \
               "buffer_load_dwordx4 %4, %1, %3 offen nt lds\n\t"                                \
               "buffer_load_dwordx4 %5, %1, %3 offen offset:1024 nt lds\n\t"                    \
               "buffer_load_dwordx4 %6, %1, %3 offen offset:2048 nt lds\n\t"                    \
               "buffer_load_dwordx4 %7, %1, %3 offen offset:3072 nt lds\n\t"                    \
               "s_add_u32 m0, m0, 0x1000\n\ts_nop 0\n\t"                                        \
               "buffer_load_dwordx4 %8, %2, %3 offen nt lds\n\t"                                \
               "buffer_load_dwordx4 %9, %2, %3 offen offset:1024 nt lds\n\t"                    \
               "buffer_load_dwordx4 %10, %2, %3 offen offset:2048 nt lds\n\t"                   \
               "buffer_load_dwordx4 %11, %2, %3 offen offset:3072 nt lds"                       \
               :: "s"(m0k), "s"(rk), "s"(rv), "s"(soff), "v"(ks0), "v"(ks1), "v"(ks2), "v"(ks3), "v"(vs0), "v"(vs1), "v"(vs2), "v"(vs3) \
               : "memory", "scc")

__global__ void __launch_bounds__(320, 2) gqa_decode(const char* __restrict__ K, const char* __restrict__ V, const char* __restrict__ Q, char* ws, unsigned* __restrict__ out) {
  extern __shared__ __attribute__((aligned(16))) char lds[];
  STAMP(0);
  const unsigned bid = blockIdx.x, pair = (bid >> 3) * 2 + ((bid >> 1) & 1), j = (bid & 1) | ((bid >> 1) & 2);
  const unsigned lane = threadIdx.x & 63, w = __builtin_amdgcn_readfirstlane(threadIdx.x >> 6);
  const unsigned g = lane >> 4, c = lane & 15;
  constexpr unsigned PX = LAY ? 3072 : 0, WSTEP = LAY ? 1024 : 4096;
  unsigned ks0 = g * 256 + ((c ^ g) << 4), ks1 = (ks0 ^ 64) + PX, ks2 = (ks0 ^ 128) + 2 * PX, ks3 = (ks0 ^ 192) + 3 * PX;
  unsigned vs0 = g * 256 + ((c ^ (g << 2)) << 4), vs1 = (vs0 ^ 32) + PX, vs2 = vs0 + 2 * PX, vs3 = (vs0 ^ 32) + 3 * PX;
  unsigned qo = (c & 3) * 256 + g * 16;
  const unsigned ldsb = (unsigned)(size_t)lds;
  const unsigned ring = ldsb + w * RING_BYTES;
  unsigned soff = j * UNIT_BYTES + w * WSTEP;
  asm volatile("" : "+v"(ks0), "+v"(ks1), "+v"(ks2), "+v"(ks3), "+v"(vs0), "+v"(vs1), "+v"(vs2), "+v"(vs3), "+v"(qo) : "s"(ring), "s"(soff));
  asm volatile("" :: "s"(K), "s"(V), "s"(Q), "s"(ws), "s"(out));
  char* const gws = ws + (size_t)pair * G_WS;
  if (w == NW) {
    volatile lu32* const tk = (volatile lu32*)(lds + TK_OFF);
    tk[lane] = lane == 63 ? 0u : NOTREADY;
    asm volatile("s_waitcnt lgkmcnt(0)" ::: "memory");
    __builtin_amdgcn_s_barrier();
    __builtin_amdgcn_s_setprio(3);
    gu32* const ctl = (gu32*)(gws + WS_CTL);
    const unsigned i1 = (j + 1) & 3, i2 = (j + 2) & 3, i3 = (j + 3) & 3;
    unsigned e0; u32x4 a1, a2, a3, b1, b2, b3;
    {
      const unsigned lo = lane * 16;
      asm volatile("global_load_dword %0, %7, %14 sc1\n\t"
                   "global_load_dwordx4 %1, %8, %14 sc1\n\tglobal_load_dwordx4 %2, %9, %14 sc1\n\tglobal_load_dwordx4 %3, %10, %14 sc1\n\t"
                   "global_load_dwordx4 %4, %11, %14 sc1\n\tglobal_load_dwordx4 %5, %12, %14 sc1\n\tglobal_load_dwordx4 %6, %13, %14 sc1\n\t"
                   "s_waitcnt vmcnt(0)"
                   : "=&v"(e0), "=&v"(a1), "=&v"(a2), "=&v"(a3), "=&v"(b1), "=&v"(b2), "=&v"(b3)
                   : "v"(WS_CTL + (lane & 7) * 4), "v"(lo + j * 4096 + i1 * 1024), "v"(lo + j * 4096 + i2 * 1024), "v"(lo + j * 4096 + i3 * 1024),
                     "v"(lo + i1 * 4096 + j * 1024), "v"(lo + i2 * 4096 + j * 1024), "v"(lo + i3 * 4096 + j * 1024), "s"(gws) : "memory");
    }
    lu32* const msk = (lu32*)(lds + MSK_OFF);
    msk[j * 64 + lane] = lsb4(a1) | (lsb4(a2) << 4) | (lsb4(a3) << 8);
    msk[i1 * 64 + lane] = lsb4(b1); msk[i2 * 64 + lane] = lsb4(b2); msk[i3 * 64 + lane] = lsb4(b3);
    const unsigned c0 = __builtin_amdgcn_readlane(e0, 4);
    __builtin_amdgcn_s_sleep(100);
#pragma unroll 1
    for (unsigned n = 0; n < SPIN_CAP; ++n) {
      if ((n & 3) == 0 && lane == 0) __hip_atomic_fetch_add(ctl + j, 1u, RLX_AGENT);
      __builtin_amdgcn_s_sleep(64);
      const unsigned e = __hip_atomic_load(ctl + (lane & 7), RLX_AGENT);
      if (__builtin_amdgcn_ballot_w64(lane < 4 && lane != j && e == e0) == 0) break;
    }
    if (DYN) {
      unsigned tv = 0;
      const char* const ctr = gws + WS_CTL + 16;
#pragma unroll 1
      for (unsigned k = 0;; ++k) {
#pragma unroll 1
        for (unsigned n = 0; n < SPIN_CAP; ++n) {
          if (__builtin_amdgcn_readfirstlane(tk[63]) >= S_IT - NSLOT - LEAD + k) break;
          __builtin_amdgcn_s_sleep(2);
        }
        asm volatile("s_lshl_b64 exec, 1, %1\n\tglobal_atomic_add %0, %2, %3, %4 sc0\n\ts_mov_b64 exec, -1\n\ts_waitcnt vmcnt(1)"
                     : "+v"(tv) : "s"(k), "v"(0u), "v"(1u), "s"(ctr) : "memory", "scc");
        if (k == 0) continue;
        const unsigned t = min(__builtin_amdgcn_readlane(tv, (k + 63) & 63) - c0, NDYN);
        tk[(k + 63) & 63] = t;
        if (t == NDYN) break;
      }
      asm volatile("s_waitcnt vmcnt(0)" : "+v"(tv) :: "memory");
    }
    asm volatile("s_waitcnt lgkmcnt(0)" ::: "memory");
    __builtin_amdgcn_s_barrier();
    return;
  }
  const __amdgpu_buffer_rsrc_t rk = __builtin_amdgcn_make_buffer_rsrc((void*)(K + (size_t)pair * PAIR_BYTES), 0, PAIR_BYTES, 0x00020000);
  const __amdgpu_buffer_rsrc_t rv = __builtin_amdgcn_make_buffer_rsrc((void*)(V + (size_t)pair * PAIR_BYTES), 0, PAIR_BYTES, 0x00020000);
  STAMP(1);
  u32x4 q0, q1, q2, q3;
  DMA_TILE(ring, soff); soff += GRP * UNIT_BYTES;
  {
    const char* qp = Q + (size_t)pair * 1024;
    asm volatile("global_load_dwordx4 %0, %4, %5\n\tglobal_load_dwordx4 %1, %4, %5 offset:64\n\t"
                 "global_load_dwordx4 %2, %4, %5 offset:128\n\tglobal_load_dwordx4 %3, %4, %5 offset:192"
                 : "=&v"(q0), "=&v"(q1), "=&v"(q2), "=&v"(q3) : "v"(qo), "s"(qp) : "memory");
  }
#pragma unroll
  for (int s = 1; s < NSLOT; ++s) { const unsigned m0k = ring + s * SLOT_BYTES; DMA_TILE(m0k, soff); soff += GRP * UNIT_BYTES; }
  __builtin_amdgcn_s_barrier();
  asm volatile("s_waitcnt vmcnt(%4)" : "+v"(q0), "+v"(q1), "+v"(q2), "+v"(q3) : "i"(8 * (NSLOT - 1)) : "memory");

  const unsigned ka = c * 256 + ((g ^ c) << 4);
  const unsigned qq = (lane >> 2) & 3, pp = lane & 3;
  const unsigned va = TILE_BYTES + (4 * g + qq) * 256 + (((pp >> 1) ^ (qq << 2) ^ ((g & 1) << 1)) << 4) + 8 * (pp & 1);
  float m = -INFINITY, l = 0.f;
  f32x4 acc[8];
#pragma unroll
  for (int b = 0; b < 8; ++b) acc[b] = f32x4{0.f, 0.f, 0.f, 0.f};
  unsigned slot = ring;
  int infl = NSLOT;
#pragma unroll 1
  for (int it = 0;; ++it) {
    u32x4 k0, k1, k2, k3; u32x2 v0, v1, v2, v3, v4, v5, v6, v7; unsigned tkv;
    const unsigned a = slot + ka, b0 = slot + va;
    const unsigned tka = ldsb + TK_OFF + 4 * (unsigned)max(it + NSLOT - S_IT, 0);
    if (infl == NSLOT) asm volatile("s_waitcnt vmcnt(%0)" :: "i"(8 * (NSLOT - 1)) : "memory");
    else if (NSLOT > 2 && infl == 2) asm volatile("s_waitcnt vmcnt(8)" ::: "memory");
    else asm volatile("s_waitcnt vmcnt(0)" ::: "memory");
    asm volatile("ds_read_b128 %0, %13\n\tds_read_b128 %1, %14\n\tds_read_b128 %2, %15\n\tds_read_b128 %3, %16\n\t"
                 "ds_read_b64_tr_b16 %4, %17\n\tds_read_b64_tr_b16 %5, %18\n\tds_read_b64_tr_b16 %6, %19\n\tds_read_b64_tr_b16 %7, %20\n\t"
                 "ds_read_b64_tr_b16 %8, %21\n\tds_read_b64_tr_b16 %9, %22\n\tds_read_b64_tr_b16 %10, %23\n\tds_read_b64_tr_b16 %11, %24\n\t"
                 "s_mov_b64 exec, 1\n\tds_max_u32 %26, %27\n\tds_read_b32 %12, %25\n\ts_mov_b64 exec, -1\n\t"
                 "s_waitcnt lgkmcnt(0)"
                 : "=&v"(k0), "=&v"(k1), "=&v"(k2), "=&v"(k3), "=&v"(v0), "=&v"(v1), "=&v"(v2), "=&v"(v3), "=&v"(v4), "=&v"(v5), "=&v"(v6), "=&v"(v7), "=&v"(tkv)
                 : "v"(a), "v"(a ^ 64), "v"(a ^ 128), "v"(a ^ 192),
                   "v"(b0), "v"(b0 ^ 32), "v"(b0 ^ 64), "v"(b0 ^ 96), "v"(b0 ^ 128), "v"(b0 ^ 160), "v"(b0 ^ 192), "v"(b0 ^ 224),
                   "v"(tka), "v"(ldsb + TK_OFF + 252), "v"(it)
                 : "memory");
    bool go = it + NSLOT < S_IT;
    if (DYN && !go && infl == NSLOT) {
      unsigned n = __builtin_amdgcn_readfirstlane(tkv);
#pragma unroll 1
      for (unsigned s = 0; n == NOTREADY && s < SPIN_CAP; ++s) n = __builtin_amdgcn_readfirstlane(*(volatile lu32*)(size_t)tka);
      go = n < NDYN;
      soff = (S_IT * GRP + n) * UNIT_BYTES + w * WSTEP;
    }
    if (go) { DMA_TILE(slot, soff); soff += GRP * UNIT_BYTES; } else --infl;
    slot += SLOT_BYTES; if (slot == ring + RING_BYTES) slot = ring;
    f32x4 s = {0.f, 0.f, 0.f, 0.f};
    s = __builtin_amdgcn_mfma_f32_16x16x32_bf16(__builtin_bit_cast(bf16x8, k0), __builtin_bit_cast(bf16x8, q0), s, 0, 0, 0);
    s = __builtin_amdgcn_mfma_f32_16x16x32_bf16(__builtin_bit_cast(bf16x8, k1), __builtin_bit_cast(bf16x8, q1), s, 0, 0, 0);
    s = __builtin_amdgcn_mfma_f32_16x16x32_bf16(__builtin_bit_cast(bf16x8, k2), __builtin_bit_cast(bf16x8, q2), s, 0, 0, 0);
    s = __builtin_amdgcn_mfma_f32_16x16x32_bf16(__builtin_bit_cast(bf16x8, k3), __builtin_bit_cast(bf16x8, q3), s, 0, 0, 0);
    const float tm = fmaxf(fmaxf(s[0], s[1]), fmaxf(s[2], s[3])) * C2;
    if (__builtin_amdgcn_ballot_w64(tm > m) != 0) {
      const float mn = fmaxf(m, xg_max(tm));
      const float al = __builtin_amdgcn_exp2f(m - mn);
      m = mn; l *= al;
#pragma unroll
      for (int b = 0; b < 8; ++b) acc[b] *= al;
    }
    const float p0 = __builtin_amdgcn_exp2f(fmaf(s[0], C2, -m)), p1 = __builtin_amdgcn_exp2f(fmaf(s[1], C2, -m));
    const float p2 = __builtin_amdgcn_exp2f(fmaf(s[2], C2, -m)), p3 = __builtin_amdgcn_exp2f(fmaf(s[3], C2, -m));
    l += (p0 + p1) + (p2 + p3);
    const u32x2 pk = {cvtpk(p0, p1), cvtpk(p2, p3)};
    const s16x4 pb = __builtin_bit_cast(s16x4, pk);
    acc[0] = __builtin_amdgcn_mfma_f32_16x16x16bf16_1k(__builtin_bit_cast(s16x4, v0), pb, acc[0], 0, 0, 0);
    acc[1] = __builtin_amdgcn_mfma_f32_16x16x16bf16_1k(__builtin_bit_cast(s16x4, v1), pb, acc[1], 0, 0, 0);
    acc[2] = __builtin_amdgcn_mfma_f32_16x16x16bf16_1k(__builtin_bit_cast(s16x4, v2), pb, acc[2], 0, 0, 0);
    acc[3] = __builtin_amdgcn_mfma_f32_16x16x16bf16_1k(__builtin_bit_cast(s16x4, v3), pb, acc[3], 0, 0, 0);
    acc[4] = __builtin_amdgcn_mfma_f32_16x16x16bf16_1k(__builtin_bit_cast(s16x4, v4), pb, acc[4], 0, 0, 0);
    acc[5] = __builtin_amdgcn_mfma_f32_16x16x16bf16_1k(__builtin_bit_cast(s16x4, v5), pb, acc[5], 0, 0, 0);
    acc[6] = __builtin_amdgcn_mfma_f32_16x16x16bf16_1k(__builtin_bit_cast(s16x4, v6), pb, acc[6], 0, 0, 0);
    acc[7] = __builtin_amdgcn_mfma_f32_16x16x16bf16_1k(__builtin_bit_cast(s16x4, v7), pb, acc[7], 0, 0, 0);
    if (infl == 0) break;
  }
  STAMP(3);
  l = xg_sum(l);
  char* red = lds + RED_OFF;
  if (c < 4) {
    char* rec = red + (w * 4 + c) * RED_REC;
#pragma unroll
    for (int b = 0; b < 8; ++b) *(f32x4*)(rec + (16 * b + 4 * g) * 4) = acc[b];
    if (g == 0) *(f32x2*)(rec + DH * 4) = f32x2{m, l};
  }
  asm volatile("s_waitcnt lgkmcnt(0)" ::: "memory");
  __builtin_amdgcn_s_barrier();
  asm volatile("" ::: "memory");
  u32x4 r;
  const unsigned stale = *(const unsigned*)(lds + MSK_OFF + (w * 64 + lane) * 4);
  {
    f32x2 ml[NW], o[NW];
#pragma unroll
    for (int x = 0; x < NW; ++x) { const char* rec = red + (x * 4 + w) * RED_REC; ml[x] = *(const f32x2*)(rec + DH * 4); o[x] = *(const f32x2*)(rec + lane * 8); }
    const float M = __uint_as_float(__float_as_uint(fmaxf(fmaxf(ml[0][0], ml[1][0]), fmaxf(ml[2][0], ml[3][0]))) & ~1u);
    float L = 0.f, o0 = 0.f, o1 = 0.f;
#pragma unroll
    for (int x = 0; x < NW; ++x) { const float a = __builtin_amdgcn_exp2f(ml[x][0] - M); L = fmaf(a, ml[x][1], L); o0 = fmaf(a, o[x][0], o0); o1 = fmaf(a, o[x][1], o1); }
    r = u32x4{__float_as_uint(o0) & ~1u, __float_as_uint(o1) & ~1u, __float_as_uint(M), __float_as_uint(L) & ~1u};
  }
  STAMP(4);
  if (w != j) {
    const unsigned f = ~stale;
    const u32x4 u = {r[0] | (f & 1), r[1] | ((f >> 1) & 1), r[2] | ((f >> 2) & 1), r[3] | ((f >> 3) & 1)};
    const __amdgpu_buffer_rsrc_t rm = __builtin_amdgcn_make_buffer_rsrc((void*)gws, 0, WS_CTL, 0x00020000);
    __builtin_amdgcn_raw_buffer_store_b128(u, rm, lane * 16 + w * 4096 + j * 1024, 0, 16);
  } else {
    const unsigned ob = lane * 16 + j * 4096;
    const unsigned o1 = ob + ((j + 1) & 3) * 1024, o2 = ob + ((j + 2) & 3) * 1024, o3 = ob + ((j + 3) & 3) * 1024;
    u32x4 x1, x2, x3;
#pragma unroll 1
    for (unsigned n = 1; n < SPIN_CAP; ++n) {
      asm volatile("global_load_dwordx4 %0, %3, %6 sc1\n\tglobal_load_dwordx4 %1, %4, %6 sc1\n\tglobal_load_dwordx4 %2, %5, %6 sc1\n\ts_waitcnt vmcnt(0)"
                   : "=&v"(x1), "=&v"(x2), "=&v"(x3) : "v"(o1), "v"(o2), "v"(o3), "s"(gws) : "memory");
      const unsigned fresh = (lsb4(x1) | (lsb4(x2) << 4) | (lsb4(x3) << 8)) ^ stale;
      if (__builtin_amdgcn_ballot_w64(fresh != 0xFFFu) == 0) break;
      if ((n & 31) == 0 && lane == 0) __hip_atomic_fetch_add((gu32*)(gws + WS_CTL) + j, 1u, RLX_AGENT);
    }
    const f32x4 r0 = __builtin_bit_cast(f32x4, r), r1 = __builtin_bit_cast(f32x4, x1 & ~1u), r2 = __builtin_bit_cast(f32x4, x2 & ~1u), r3 = __builtin_bit_cast(f32x4, x3 & ~1u);
    const float M = fmaxf(fmaxf(r0[2], r1[2]), fmaxf(r2[2], r3[2]));
    const float a0 = __builtin_amdgcn_exp2f(r0[2] - M), a1 = __builtin_amdgcn_exp2f(r1[2] - M), a2 = __builtin_amdgcn_exp2f(r2[2] - M), a3 = __builtin_amdgcn_exp2f(r3[2] - M);
    const float L = fmaf(a0, r0[3], fmaf(a1, r1[3], fmaf(a2, r2[3], a3 * r3[3])));
    const float y0 = fmaf(a0, r0[0], fmaf(a1, r1[0], fmaf(a2, r2[0], a3 * r3[0])));
    const float y1 = fmaf(a0, r0[1], fmaf(a1, r1[1], fmaf(a2, r2[1], a3 * r3[1])));
    const float inv = 1.f / L;
    out[(pair * 4 + j) * 64 + lane] = cvtpk(y0 * inv, y1 * inv);
  }
}

extern "C" void kernel_launch(void* const* d_in, const int* in_sizes, int n_in, void* d_out, int out_size, void* d_ws, size_t ws_size, hipStream_t stream) {
  (void)hipFuncSetAttribute((const void*)gqa_decode, hipFuncAttributeMaxDynamicSharedMemorySize, LDS_BYTES);
  gqa_decode<<<NPAIR * GRP, 320, LDS_BYTES, stream>>>((const char*)d_in[1], (const char*)d_in[2], (const char*)d_in[0], (char*)d_ws, (unsigned*)d_out);
}
```

```cpp
#include <hip/hip_runtime.h>
#include <hip/hip_bf16.h>

typedef unsigned u32x4 __attribute__((ext_vector_type(4)));
typedef unsigned u32x2 __attribute__((ext_vector_type(2)));
typedef float f32x4 __attribute__((ext_vector_type(4)));
typedef float f32x2 __attribute__((ext_vector_type(2)));
typedef __bf16 bf16x8 __attribute__((ext_vector_type(8)));
typedef __bf16 bf16x2 __attribute__((ext_vector_type(2)));
typedef short s16x4 __attribute__((ext_vector_type(4)));
typedef __attribute__((address_space(1))) unsigned gu32;
typedef __attribute__((address_space(3))) unsigned lu32;
#define RLX_AGENT __ATOMIC_RELAXED, __HIP_MEMORY_SCOPE_AGENT

#define STAMP(i)
#define DYN 1
#define NSTAT 26
#define LEAD 2
#define NSEP (S_IT - 1)
#define XW 8
#define TOUCH 1
#define CODEPF 0
#define NSLOT 3
constexpr int SK = 8192, DH = 128, NPAIR = 64, GRP = 4, NW = 4;
constexpr int TILE = 16;
constexpr int NUNIT = SK / (TILE * NW);
constexpr int S_IT = DYN ? NSTAT : NUNIT / GRP;
constexpr int S_SEP = NSEP;
constexpr unsigned NDYN = NUNIT - S_IT * GRP;
static_assert(S_SEP >= 2 && S_SEP < S_IT && S_SEP <= 32, "");
static_assert(NDYN < 60, "ticket table is one LDS word per lane");
constexpr unsigned PAIR_BYTES = SK * DH * 2;
constexpr unsigned TILE_BYTES = TILE * DH * 2;
constexpr unsigned UNIT_BYTES = NW * TILE_BYTES;
constexpr unsigned RING_BYTES = NSLOT * TILE_BYTES;
constexpr unsigned SC_OFF = NW * RING_BYTES;
constexpr unsigned SC_BYTES = 32 * 256;
constexpr unsigned RED_OFF = SC_OFF + NW * SC_BYTES;
constexpr unsigned RED_REC = (DH + 4) * 4;
constexpr unsigned TK_OFF = RED_OFF + NW * 4 * RED_REC;
constexpr unsigned MSK_OFF = TK_OFF + 256;
constexpr unsigned LDS_BYTES = MSK_OFF + 1024 > 81 * 1024 ? MSK_OFF + 1024 : 81 * 1024;

constexpr float C2 = 0.088388347648318440f * 1.4426950408889634f;
constexpr unsigned G_WS = 32768, WS_CTL = 16384;
constexpr unsigned NOTREADY = 0xFFFFFFFFu;
constexpr unsigned SPIN_CAP = 1u << 20;

__device__ __forceinline__ unsigned cvtpk(float lo, float hi) {
  bf16x2 v = {(__bf16)lo, (__bf16)hi};
  return __builtin_bit_cast(unsigned, v);
}
__device__ __forceinline__ float xg_max(float x) {
  auto s = __builtin_amdgcn_permlane16_swap(__float_as_uint(x), __float_as_uint(x), false, false);
  x = fmaxf(__uint_as_float(s[0]), __uint_as_float(s[1]));
  auto t = __builtin_amdgcn_permlane32_swap(__float_as_uint(x), __float_as_uint(x), false, false);
  return fmaxf(__uint_as_float(t[0]), __uint_as_float(t[1]));
}
__device__ __forceinline__ float xg_sum(float x) {
  auto s = __builtin_amdgcn_permlane16_swap(__float_as_uint(x), __float_as_uint(x), false, false);
  x = __uint_as_float(s[0]) + __uint_as_float(s[1]);
  auto t = __builtin_amdgcn_permlane32_swap(__float_as_uint(x), __float_as_uint(x), false, false);
  return __uint_as_float(t[0]) + __uint_as_float(t[1]);
}
__device__ __forceinline__ unsigned lsb4(u32x4 x) { return (x[0] & 1) | ((x[1] & 1) << 1) | ((x[2] & 1) << 2) | ((x[3] & 1) << 3); }

#define DMA_TILE(rs, m0v, soff)                                                                 \
  asm volatile("s_mov_b32 m0, %0\n\ts_nop 0\n\t"                                                \
               "buffer_load_dwordx4 %3, %1, %2 offen nt lds\n\t"                                \
               "buffer_load_dwordx4 %4, %1, %2 offen offset:1024 nt lds\n\t"                    \
               "buffer_load_dwordx4 %5, %1, %2 offen offset:2048 nt lds\n\t"                    \
               "buffer_load_dwordx4 %6, %1, %2 offen offset:3072 nt lds"                        \
               :: "s"(m0v), "s"(rs), "s"(soff), "v"(so0), "v"(so1), "v"(so2), "v"(so3) : "memory")

__global__ void __launch_bounds__(320, 2) gqa_decode(const char* __restrict__ K, const char* __restrict__ V, const char* __restrict__ Q, char* ws, unsigned* __restrict__ out) {
  extern __shared__ __attribute__((aligned(16))) char lds[];
  STAMP(0);
  unsigned pfd = 0;
  if (CODEPF) {
    unsigned long long pc;
    asm volatile("s_getpc_b64 %0" : "=s"(pc));
    asm volatile("global_load_dword %0, %1, %2" : "=v"(pfd) : "v"(min(threadIdx.x * 32, (unsigned)CODEPF)), "s"(pc) : "memory");
  }
  const unsigned bid = blockIdx.x, pair = (bid >> 3) * 2 + ((bid >> 1) & 1), j = (bid & 1) | ((bid >> 1) & 2);
  const unsigned lane = threadIdx.x & 63, w = __builtin_amdgcn_readfirstlane(threadIdx.x >> 6);
  const unsigned g = lane >> 4, c = lane & 15;
  unsigned so0 = g * 256 + ((c ^ (g << 2)) << 4), so1 = (so0 ^ 32) + 3072, so2 = (so0 ^ 48) + 2 * 3072, so3 = (so0 ^ 16) + 3 * 3072;
  unsigned qo = (c & 3) * 256 + g * 16;
  const unsigned ldsb = (unsigned)(size_t)lds;
  const unsigned ring = ldsb + w * RING_BYTES;
  const unsigned soff0 = j * UNIT_BYTES + w * 1024;
  asm volatile("" : "+v"(so0), "+v"(so1), "+v"(so2), "+v"(so3), "+v"(qo) : "s"(ring), "s"(soff0));
  asm volatile("" :: "s"(K), "s"(V), "s"(Q), "s"(ws), "s"(out));
  char* const gws = ws + (size_t)pair * G_WS;
  if (w == NW) {
    volatile lu32* const tk = (volatile lu32*)(lds + TK_OFF);
    tk[lane] = lane == 63 ? 0u : NOTREADY;
    asm volatile("s_waitcnt lgkmcnt(0)" ::: "memory");
    __builtin_amdgcn_s_barrier();
    __builtin_amdgcn_s_setprio(3);
    asm volatile("s_waitcnt vmcnt(0)" : "+v"(pfd) :: "memory");
    if (TOUCH) {
      unsigned d0, d1;
      asm volatile("global_load_dword %0, %2, %3 nt\n\tglobal_load_dword %1, %2, %4 nt\n\ts_waitcnt vmcnt(0)" : "=&v"(d0), "=&v"(d1)
                   : "v"(0u), "s"(V + (size_t)pair * PAIR_BYTES + soff0), "s"(out + (pair * 4 + j) * 64) : "memory");
    }
    gu32* const ctl = (gu32*)(gws + WS_CTL);
    const unsigned i1 = (j + 1) & 3, i2 = (j + 2) & 3, i3 = (j + 3) & 3;
    unsigned e0; u32x4 a1, a2, a3, b1, b2, b3;
    {
      const unsigned lo = lane * 16;
      asm volatile("global_load_dword %0, %7, %14 sc1\n\t"
                   "global_load_dwordx4 %1, %8, %14 sc1\n\tglobal_load_dwordx4 %2, %9, %14 sc1\n\tglobal_load_dwordx4 %3, %10, %14 sc1\n\t"
                   "global_load_dwordx4 %4, %11, %14 sc1\n\tglobal_load_dwordx4 %5, %12, %14 sc1\n\tglobal_load_dwordx4 %6, %13, %14 sc1\n\t"
                   "s_waitcnt vmcnt(0)"
                   : "=&v"(e0), "=&v"(a1), "=&v"(a2), "=&v"(a3), "=&v"(b1), "=&v"(b2), "=&v"(b3)
                   : "v"(WS_CTL + (lane & 7) * 4), "v"(lo + j * 4096 + i1 * 1024), "v"(lo + j * 4096 + i2 * 1024), "v"(lo + j * 4096 + i3 * 1024),
                     "v"(lo + i1 * 4096 + j * 1024), "v"(lo + i2 * 4096 + j * 1024), "v"(lo + i3 * 4096 + j * 1024), "s"(gws) : "memory");
    }
    lu32* const msk = (lu32*)(lds + MSK_OFF);
    msk[j * 64 + lane] = lsb4(a1) | (lsb4(a2) << 4) | (lsb4(a3) << 8);
    msk[i1 * 64 + lane] = lsb4(b1); msk[i2 * 64 + lane] = lsb4(b2); msk[i3 * 64 + lane] = lsb4(b3);
    const unsigned c0 = __builtin_amdgcn_readlane(e0, 4);
    __builtin_amdgcn_s_sleep(100);
#pragma unroll 1
    for (unsigned n = 0; n < SPIN_CAP; ++n) {
      if ((n & 3) == 0 && lane == 0) __hip_atomic_fetch_add(ctl + j, 1u, RLX_AGENT);
      __builtin_amdgcn_s_sleep(64);
      const unsigned e = __hip_atomic_load(ctl + (lane & 7), RLX_AGENT);
      if (__builtin_amdgcn_ballot_w64(lane < 4 && lane != j && e == e0) == 0) break;
    }
    if (DYN) {
#pragma unroll 1
      for (unsigned k = 0;; ++k) {
#pragma unroll 1
        for (unsigned n = 0; n < SPIN_CAP; ++n) {
          if (__builtin_amdgcn_readfirstlane(tk[63]) >= 2 * (S_IT - 1) - LEAD + 2 * k) break;
          __builtin_amdgcn_s_sleep(1);
        }
        unsigned tv = 0;
        if (lane == 0) tv = __hip_atomic_fetch_add(ctl + 4, 1u, RLX_AGENT);
        const unsigned t = min(__builtin_amdgcn_readfirstlane(tv) - c0, NDYN);
        tk[k] = t;
        if (t == NDYN) break;
      }
    }
    asm volatile("s_waitcnt lgkmcnt(0)" ::: "memory");
    __builtin_amdgcn_s_barrier();
    return;
  }
  const char* const kb = K + (size_t)pair * PAIR_BYTES;
  const char* const vb = V + (size_t)pair * PAIR_BYTES;
#define RSRC(p) __builtin_amdgcn_make_buffer_rsrc((void*)(p), 0, PAIR_BYTES, 0x00020000)
  STAMP(1);
  u32x4 q0, q1, q2, q3;
  DMA_TILE(RSRC(kb), ring, soff0);
  {
    const char* qp = Q + (size_t)pair * 1024;
    asm volatile("global_load_dwordx4 %0, %4, %5\n\tglobal_load_dwordx4 %1, %4, %5 offset:64\n\t"
                 "global_load_dwordx4 %2, %4, %5 offset:128\n\tglobal_load_dwordx4 %3, %4, %5 offset:192"
                 : "=&v"(q0), "=&v"(q1), "=&v"(q2), "=&v"(q3) : "v"(qo), "s"(qp) : "memory");
  }
  DMA_TILE(RSRC(kb), ring + TILE_BYTES, soff0 + GRP * UNIT_BYTES);
  __builtin_amdgcn_s_barrier();

  const unsigned fc = ((c & 3) << 2) | ((((c >> 2) ^ (c >> 3)) & 1) << 1) | (c >> 3);
  const unsigned ka = c * 256 + ((g ^ fc) << 4);
  const unsigned qq = (lane >> 2) & 3, pp = lane & 3;
  const unsigned fr = (qq << 2) | (((g ^ (g >> 1)) & 1) << 1) | (g >> 1);
  const unsigned va = (4 * g + qq) * 256 + (((pp >> 1) ^ fr) << 4) + 8 * (pp & 1);
  const unsigned sa = ldsb + SC_OFF + w * SC_BYTES + (c & 3) * 64 + g * 16;
  float m = -INFINITY, mrun = -INFINITY, l = 0.f;
  unsigned rd = ring, wr = ring + 2 * TILE_BYTES;
  asm volatile("s_waitcnt vmcnt(4)" : "+v"(q0), "+v"(q1), "+v"(q2), "+v"(q3), "+v"(pfd) :: "memory");

#define ADVANCE() do { wr = rd; rd += TILE_BYTES; if (rd == ring + RING_BYTES) rd = ring; } while (0)
#define ISSUE(p, off) do { const __amdgpu_buffer_rsrc_t rs_ = RSRC(p); const unsigned off_ = (off);                 \
    asm volatile("s_waitcnt vmcnt(%0)" :: "i"(XW), "s"(rs_), "s"(off_) : "memory");                                 \
    DMA_TILE(rs_, wr, off_);                                                                                        \
    if (XW > 4) asm volatile("s_waitcnt vmcnt(8)" ::: "memory"); } while (0)
#define PROGRESS(h) asm volatile("s_mov_b64 exec, 1\n\tds_max_u32 %0, %1\n\ts_mov_b64 exec, -1" :: "v"(ldsb + TK_OFF + 252), "v"(h) : "memory")
#define K_STEP(sc) do {                                                           \
    u32x4 k0, k1, k2, k3;                                                                                           \
    asm volatile("ds_read_b128 %0, %4\n\tds_read_b128 %1, %5\n\tds_read_b128 %2, %6\n\tds_read_b128 %3, %7\n\ts_waitcnt lgkmcnt(0)" \
                 : "=&v"(k0), "=&v"(k1), "=&v"(k2), "=&v"(k3) : "v"(a0), "v"(a1), "v"(a2), "v"(a3) : "memory");      \
    f32x4 s = {0.f, 0.f, 0.f, 0.f};                                                                                 \
    s = __builtin_amdgcn_mfma_f32_16x16x32_bf16(__builtin_bit_cast(bf16x8, k0), __builtin_bit_cast(bf16x8, q0), s, 0, 0, 0); \
    s = __builtin_amdgcn_mfma_f32_16x16x32_bf16(__builtin_bit_cast(bf16x8, k1), __builtin_bit_cast(bf16x8, q1), s, 0, 0, 0); \
    s = __builtin_amdgcn_mfma_f32_16x16x32_bf16(__builtin_bit_cast(bf16x8, k2), __builtin_bit_cast(bf16x8, q2), s, 0, 0, 0); \
    s = __builtin_amdgcn_mfma_f32_16x16x32_bf16(__builtin_bit_cast(bf16x8, k3), __builtin_bit_cast(bf16x8, q3), s, 0, 0, 0); \
    mrun = fmaxf(mrun, fmaxf(fmaxf(s[0], s[1]), fmaxf(s[2], s[3])) * C2);                                           \
    *(__attribute__((address_space(3))) f32x4*)(size_t)(sc) = s;                \
    ADVANCE();                                                                                                      \
  } while (0)
#define K_ADDR() unsigned a0 = rd + ka, a1 = a0 ^ 64, a2 = a0 ^ 128, a3 = a0 ^ 192; asm volatile("" : "+v"(a0), "+v"(a1), "+v"(a2), "+v"(a3))

#pragma unroll 1
  for (int h = 0; h < S_SEP; ++h) {
    K_ADDR();
    const bool nv = h + 2 >= S_SEP;
    ISSUE(nv ? vb : kb, soff0 + (unsigned)(nv ? h + 2 - S_SEP : h + 2) * (GRP * UNIT_BYTES));
    K_STEP(sa + 256 * h);
  }
  f32x4 acc[8];
#pragma unroll
  for (int b = 0; b < 8; ++b) acc[b] = f32x4{0.f, 0.f, 0.f, 0.f};
  unsigned noff = 0;
  bool more = true;
#pragma unroll 1
  for (int u = 0;; ++u) {
    if (u >= S_SEP) {
      K_ADDR();
      noff = soff0 + (unsigned)(u + 1) * (GRP * UNIT_BYTES);
      if (DYN) PROGRESS(2 * u);
      if (u + 1 >= S_IT) {
        unsigned t = NDYN;
        if (DYN) {
          volatile lu32* const tp = (volatile lu32*)(lds + TK_OFF) + (u + 1 - S_IT);
          t = NOTREADY;
#pragma unroll 1
          for (unsigned s = 0; t == NOTREADY && s < SPIN_CAP; ++s) t = __builtin_amdgcn_readfirstlane(*tp);
        }
        more = t < NDYN;
        noff = (S_IT * GRP + t) * UNIT_BYTES + w * 1024;
      }
      if (more) ISSUE(kb, noff); else asm volatile("s_waitcnt vmcnt(4)" ::: "memory");
      K_STEP(sa);
    }
    unsigned b0 = rd + va, b1 = b0 ^ 32, b2 = b0 ^ 64, b3 = b0 ^ 96, b4 = b0 ^ 128, b5 = b0 ^ 160, b6 = b0 ^ 192, b7 = b0 ^ 224;
    asm volatile("" : "+v"(b0), "+v"(b1), "+v"(b2), "+v"(b3), "+v"(b4), "+v"(b5), "+v"(b6), "+v"(b7));
    if (DYN) PROGRESS(u < S_SEP ? S_SEP + u : 2 * u + 1);
    if (__builtin_amdgcn_ballot_w64(mrun > m) != 0) {
      const float mn = xg_max(mrun);
      const float al = __builtin_amdgcn_exp2f(m - mn);
      m = mn; l *= al;
#pragma unroll
      for (int b = 0; b < 8; ++b) acc[b] *= al;
    }
    const f32x4 s = *(const __attribute__((address_space(3))) f32x4*)(size_t)(sa + 256 * (unsigned)(u < S_SEP ? u : 0));
    const float p0 = __builtin_amdgcn_exp2f(fmaf(s[0], C2, -m)), p1 = __builtin_amdgcn_exp2f(fmaf(s[1], C2, -m));
    const float p2 = __builtin_amdgcn_exp2f(fmaf(s[2], C2, -m)), p3 = __builtin_amdgcn_exp2f(fmaf(s[3], C2, -m));
    l += (p0 + p1) + (p2 + p3);
    u32x2 pk = {cvtpk(p0, p1), cvtpk(p2, p3)};
    asm volatile("" : "+v"(pk));
    if (more) {
      const bool nk = u == S_SEP - 2;
      ISSUE(nk ? kb : vb, u >= S_SEP ? noff : soff0 + (unsigned)min(u + 2, S_SEP) * (GRP * UNIT_BYTES));
    } else asm volatile("s_waitcnt vmcnt(0)" ::: "memory");
    u32x2 v0, v1, v2, v3, v4, v5, v6, v7;
    asm volatile("ds_read_b64_tr_b16 %0, %8\n\tds_read_b64_tr_b16 %1, %9\n\tds_read_b64_tr_b16 %2, %10\n\tds_read_b64_tr_b16 %3, %11\n\t"
                 "ds_read_b64_tr_b16 %4, %12\n\tds_read_b64_tr_b16 %5, %13\n\tds_read_b64_tr_b16 %6, %14\n\tds_read_b64_tr_b16 %7, %15\n\t"
                 "s_waitcnt lgkmcnt(0)"
                 : "=&v"(v0), "=&v"(v1), "=&v"(v2), "=&v"(v3), "=&v"(v4), "=&v"(v5), "=&v"(v6), "=&v"(v7)
                 : "v"(b0), "v"(b1), "v"(b2), "v"(b3), "v"(b4), "v"(b5), "v"(b6), "v"(b7) : "memory");
    const s16x4 pb = __builtin_bit_cast(s16x4, pk);
    acc[0] = __builtin_amdgcn_mfma_f32_16x16x16bf16_1k(__builtin_bit_cast(s16x4, v0), pb, acc[0], 0, 0, 0);
    acc[1] = __builtin_amdgcn_mfma_f32_16x16x16bf16_1k(__builtin_bit_cast(s16x4, v1), pb, acc[1], 0, 0, 0);
    acc[2] = __builtin_amdgcn_mfma_f32_16x16x16bf16_1k(__builtin_bit_cast(s16x4, v2), pb, acc[2], 0, 0, 0);
    acc[3] = __builtin_amdgcn_mfma_f32_16x16x16bf16_1k(__builtin_bit_cast(s16x4, v3), pb, acc[3], 0, 0, 0);
    acc[4] = __builtin_amdgcn_mfma_f32_16x16x16bf16_1k(__builtin_bit_cast(s16x4, v4), pb, acc[4], 0, 0, 0);
    acc[5] = __builtin_amdgcn_mfma_f32_16x16x16bf16_1k(__builtin_bit_cast(s16x4, v5), pb, acc[5], 0, 0, 0);
    acc[6] = __builtin_amdgcn_mfma_f32_16x16x16bf16_1k(__builtin_bit_cast(s16x4, v6), pb, acc[6], 0, 0, 0);
    acc[7] = __builtin_amdgcn_mfma_f32_16x16x16bf16_1k(__builtin_bit_cast(s16x4, v7), pb, acc[7], 0, 0, 0);
    ADVANCE();
    if (!more) break;
  }
  STAMP(3);
  l = xg_sum(l);
  char* red = lds + RED_OFF;
  if (c < 4) {
    char* rec = red + (w * 4 + c) * RED_REC;
#pragma unroll
    for (int b = 0; b < 8; ++b) *(f32x4*)(rec + (16 * b + 4 * g) * 4) = acc[b];
    if (g == 0) *(f32x2*)(rec + DH * 4) = f32x2{m, l};
  }
  asm volatile("s_waitcnt lgkmcnt(0)" ::: "memory");
  __builtin_amdgcn_s_barrier();
  asm volatile("" ::: "memory");
  u32x4 r;
  const unsigned stale = *(const unsigned*)(lds + MSK_OFF + (w * 64 + lane) * 4);
  {
    f32x2 ml[NW], o[NW];
#pragma unroll
    for (int x = 0; x < NW; ++x) { const char* rec = red + (x * 4 + w) * RED_REC; ml[x] = *(const f32x2*)(rec + DH * 4); o[x] = *(const f32x2*)(rec + lane * 8); }
    const float M = __uint_as_float(__float_as_uint(fmaxf(fmaxf(ml[0][0], ml[1][0]), fmaxf(ml[2][0], ml[3][0]))) & ~1u);
    float L = 0.f, o0 = 0.f, o1 = 0.f;
#pragma unroll
    for (int x = 0; x < NW; ++x) { const float a = __builtin_amdgcn_exp2f(ml[x][0] - M); L = fmaf(a, ml[x][1], L); o0 = fmaf(a, o[x][0], o0); o1 = fmaf(a, o[x][1], o1); }
    r = u32x4{__float_as_uint(o0) & ~1u, __float_as_uint(o1) & ~1u, __float_as_uint(M), __float_as_uint(L) & ~1u};
  }
  STAMP(4);
  if (w != j) {
    const unsigned f = ~stale;
    const u32x4 u = {r[0] | (f & 1), r[1] | ((f >> 1) & 1), r[2] | ((f >> 2) & 1), r[3] | ((f >> 3) & 1)};
    const __amdgpu_buffer_rsrc_t rm = __builtin_amdgcn_make_buffer_rsrc((void*)gws, 0, WS_CTL, 0x00020000);
    __builtin_amdgcn_raw_buffer_store_b128(u, rm, lane * 16 + w * 4096 + j * 1024, 0, 16);
  } else {
    const unsigned ob = lane * 16 + j * 4096;
    const unsigned o1 = ob + ((j + 1) & 3) * 1024, o2 = ob + ((j + 2) & 3) * 1024, o3 = ob + ((j + 3) & 3) * 1024;
    u32x4 x1, x2, x3;
#pragma unroll 1
    for (unsigned n = 1; n < SPIN_CAP; ++n) {
      asm volatile("global_load_dwordx4 %0, %3, %6 sc1\n\tglobal_load_dwordx4 %1, %4, %6 sc1\n\tglobal_load_dwordx4 %2, %5, %6 sc1\n\ts_waitcnt vmcnt(0)"
                   : "=&v"(x1), "=&v"(x2), "=&v"(x3) : "v"(o1), "v"(o2), "v"(o3), "s"(gws) : "memory");
      const unsigned fresh = (lsb4(x1) | (lsb4(x2) << 4) | (lsb4(x3) << 8)) ^ stale;
      if (__builtin_amdgcn_ballot_w64(fresh != 0xFFFu) == 0) break;
      if ((n & 31) == 0 && lane == 0) __hip_atomic_fetch_add((gu32*)(gws + WS_CTL) + j, 1u, RLX_AGENT);
    }
    const f32x4 r0 = __builtin_bit_cast(f32x4, r), r1 = __builtin_bit_cast(f32x4, x1 & ~1u), r2 = __builtin_bit_cast(f32x4, x2 & ~1u), r3 = __builtin_bit_cast(f32x4, x3 & ~1u);
    const float M = fmaxf(fmaxf(r0[2], r1[2]), fmaxf(r2[2], r3[2]));
    const float a0 = __builtin_amdgcn_exp2f(r0[2] - M), a1 = __builtin_amdgcn_exp2f(r1[2] - M), a2 = __builtin_amdgcn_exp2f(r2[2] - M), a3 = __builtin_amdgcn_exp2f(r3[2] - M);
    const float L = fmaf(a0, r0[3], fmaf(a1, r1[3], fmaf(a2, r2[3], a3 * r3[3])));
    const float y0 = fmaf(a0, r0[0], fmaf(a1, r1[0], fmaf(a2, r2[0], a3 * r3[0])));
    const float y1 = fmaf(a0, r0[1], fmaf(a1, r1[1], fmaf(a2, r2[1], a3 * r3[1])));
    const float inv = 1.f / L;
    out[(pair * 4 + j) * 64 + lane] = cvtpk(y0 * inv, y1 * inv);
  }
}

extern "C" void kernel_launch(void* const* d_in, const int* in_sizes, int n_in, void* d_out, int out_size, void* d_ws, size_t ws_size, hipStream_t stream) {
  (void)hipFuncSetAttribute((const void*)gqa_decode, hipFuncAttributeMaxDynamicSharedMemorySize, LDS_BYTES);
  gqa_decode<<<NPAIR * GRP, 320, LDS_BYTES, stream>>>((const char*)d_in[1], (const char*)d_in[2], (const char*)d_in[0], (char*)d_ws, (unsigned*)d_out);
}
```

```cpp
#include <hip/hip_runtime.h>
#include <hip/hip_bf16.h>

typedef unsigned u32x4 __attribute__((ext_vector_type(4)));
typedef unsigned u32x2 __attribute__((ext_vector_type(2)));
typedef float f32x4 __attribute__((ext_vector_type(4)));
typedef float f32x2 __attribute__((ext_vector_type(2)));
typedef __bf16 bf16x8 __attribute__((ext_vector_type(8)));
typedef __bf16 bf16x2 __attribute__((ext_vector_type(2)));
typedef short s16x4 __attribute__((ext_vector_type(4)));
#define LDSP(T, a) ((__attribute__((address_space(3))) T*)(size_t)(a))
typedef __attribute__((address_space(1))) unsigned gu32;
#define RLX_AGENT __ATOMIC_RELAXED, __HIP_MEMORY_SCOPE_AGENT

#define STAMP(i)
#define NSTAT 27
#define LEAD 2
constexpr int SK = 8192, DH = 128, NPAIR = 64, GRP = 4, NW = 4;
constexpr int TILE = 16;
constexpr int NUNIT = SK / (TILE * NW);
constexpr int S_IT = NSTAT;
constexpr int S_SEP = S_IT - 1;
constexpr unsigned NDYN = NUNIT - S_IT * GRP;
constexpr int NPOLL = 3;
static_assert(S_SEP >= 3 && S_SEP <= 32 && NDYN < 60, "score store: 32 tiles per wave; ticket table: one LDS word per lane");
constexpr unsigned PAIR_BYTES = SK * DH * 2;
constexpr unsigned TILE_BYTES = TILE * DH * 2;
constexpr unsigned UNIT_BYTES = NW * TILE_BYTES;
constexpr unsigned ROUND_BYTES = GRP * UNIT_BYTES;
constexpr unsigned RING_BYTES = 3 * TILE_BYTES;
constexpr unsigned SC_OFF = NW * RING_BYTES, SC_BYTES = 32 * 256;
constexpr unsigned RED_OFF = SC_OFF + NW * SC_BYTES;
constexpr unsigned RED_REC = (DH + 4) * 4;
constexpr unsigned TK_OFF = RED_OFF + NW * 4 * RED_REC;
constexpr unsigned MSK_OFF = TK_OFF + 256;
constexpr unsigned LDS_BYTES = MSK_OFF + 1024;
static_assert(LDS_BYTES > 80 * 1024 && LDS_BYTES <= 160 * 1024, "exactly one workgroup per CU");
constexpr unsigned G_WS = 32768, WS_CTL = 16384;
constexpr unsigned NOTREADY = 0xFFFFFFFFu, FAILED = 1u << 31;
constexpr unsigned BEAT_CAP = 1u << 18, POLL_CAP = 1u << 21;
constexpr float C2 = 0.088388347648318440f * 1.4426950408889634f;

__device__ __forceinline__ unsigned cvtpk(float lo, float hi) {
  bf16x2 v = {(__bf16)lo, (__bf16)hi};
  return __builtin_bit_cast(unsigned, v);
}
__device__ __forceinline__ float xg_max(float x) {
  auto s = __builtin_amdgcn_permlane16_swap(__float_as_uint(x), __float_as_uint(x), false, false);
  x = fmaxf(__uint_as_float(s[0]), __uint_as_float(s[1]));
  auto t = __builtin_amdgcn_permlane32_swap(__float_as_uint(x), __float_as_uint(x), false, false);
  return fmaxf(__uint_as_float(t[0]), __uint_as_float(t[1]));
}
__device__ __forceinline__ float xg_sum(float x) {
  auto s = __builtin_amdgcn_permlane16_swap(__float_as_uint(x), __float_as_uint(x), false, false);
  x = __uint_as_float(s[0]) + __uint_as_float(s[1]);
  auto t = __builtin_amdgcn_permlane32_swap(__float_as_uint(x), __float_as_uint(x), false, false);
  return __uint_as_float(t[0]) + __uint_as_float(t[1]);
}
__device__ __forceinline__ unsigned lsb4(u32x4 x) { return (x[0] & 1) | ((x[1] & 1) << 1) | ((x[2] & 1) << 2) | ((x[3] & 1) << 3); }

#define DMA_TILE(p, m0v, soff)                                                                  \
  asm volatile("s_mov_b32 m0, %0\n\ts_nop 0\n\t"                                                \
               "buffer_load_dwordx4 %3, %1, %2 offen nt lds\n\t"                                \
               "buffer_load_dwordx4 %4, %1, %2 offen offset:1024 nt lds\n\t"                    \
               "buffer_load_dwordx4 %5, %1, %2 offen offset:2048 nt lds\n\t"                    \
               "buffer_load_dwordx4 %6, %1, %2 offen offset:3072 nt lds"                        \
               :: "s"(m0v), "s"(__builtin_amdgcn_make_buffer_rsrc((void*)(p), 0, PAIR_BYTES, 0x00020000)), "s"(soff), \
                  "v"(so0), "v"(so1), "v"(so2), "v"(so3) : "memory")
#define WAIT_VM(n) asm volatile("s_waitcnt vmcnt(" #n ")" ::: "memory")
#define LOAD3(x1, x2, x3) "global_load_dwordx4 " x1 " sc1\n\tglobal_load_dwordx4 " x2 " sc1\n\tglobal_load_dwordx4 " x3 " sc1\n\t"

__global__ void __launch_bounds__(320, 2) gqa_decode(const char* __restrict__ K, const char* __restrict__ V, const char* __restrict__ Q, char* ws, unsigned* __restrict__ out) {
  extern __shared__ __attribute__((aligned(256))) char lds[];
  STAMP(0);
  const unsigned bid = blockIdx.x, pair = (bid >> 3) * 2 + (((bid >> 1) ^ (bid & (bid >> 3))) & 1), j = (bid & 1) | ((bid >> 1) & 2);
  const unsigned lane = threadIdx.x & 63, w = __builtin_amdgcn_readfirstlane(threadIdx.x >> 6);
  const unsigned g = lane >> 4, c = lane & 15;
  unsigned so0 = g * 256 + ((c ^ (g << 2)) << 4), so1 = (so0 ^ 32) + 3072, so2 = (so0 ^ 48) + 2 * 3072, so3 = (so0 ^ 16) + 3 * 3072;
  unsigned qo = (c & 3) * 256 + g * 16;
  const unsigned ldsb = (unsigned)(size_t)lds;
  const unsigned ring = ldsb + w * RING_BYTES;
  const unsigned soff0 = j * UNIT_BYTES + w * 1024;
  asm volatile("" : "+v"(so0), "+v"(so1), "+v"(so2), "+v"(so3), "+v"(qo) : "s"(ring), "s"(soff0));
  asm volatile("" :: "s"(K), "s"(V), "s"(Q), "s"(ws), "s"(out));
  char* const gws = ws + (size_t)pair * G_WS;

  if (w == NW) {
    volatile __attribute__((address_space(3))) unsigned* const tk = LDSP(volatile unsigned, ldsb + TK_OFF);
    tk[lane] = lane == 63 ? 0u : NOTREADY;
    asm volatile("s_waitcnt lgkmcnt(0)\n\ts_barrier" ::: "memory");
    __builtin_amdgcn_s_setprio(3);
    gu32* const ctl = (gu32*)(gws + WS_CTL);
    const unsigned mo = lane * 16 + j * 4096 + j * 1024;
    unsigned e0; u32x4 a1, a2, a3, b1, b2, b3;
    asm volatile("global_load_dword %0, %7, %14 sc1\n\t" LOAD3("%1, %8, %14", "%2, %9, %14", "%3, %10, %14")
                 LOAD3("%4, %11, %14", "%5, %12, %14", "%6, %13, %14") "s_waitcnt vmcnt(0)"
                 : "=&v"(e0), "=&v"(a1), "=&v"(a2), "=&v"(a3), "=&v"(b1), "=&v"(b2), "=&v"(b3)
                 : "v"(WS_CTL + (lane & 7) * 4), "v"(mo ^ 1024), "v"(mo ^ 2048), "v"(mo ^ 3072), "v"(mo ^ 4096), "v"(mo ^ 8192), "v"(mo ^ 12288), "s"(gws)
                 : "memory");
    const unsigned c0 = __builtin_amdgcn_readlane(e0, 4);
    __builtin_amdgcn_s_sleep(64);
    if (lane == 0) __hip_atomic_fetch_add(ctl + j, 1u, RLX_AGENT);
    unsigned fail = FAILED;
#pragma unroll 1
    for (unsigned n = 0; n < BEAT_CAP; ++n) {
      __builtin_amdgcn_s_sleep(64);
      const unsigned e = __hip_atomic_load(ctl + (lane & 7), RLX_AGENT);
      if (__builtin_amdgcn_ballot_w64(lane < 4 && lane != j && e == e0) == 0) { fail = 0; break; }
    }
    if (lane == 0) __hip_atomic_fetch_add(ctl + j, 1u, RLX_AGENT);
    const unsigned ma = ldsb + MSK_OFF + lane * 4;
    *LDSP(unsigned, ma + j * 256) = lsb4(a1) | (lsb4(a2) << 4) | (lsb4(a3) << 8) | fail;
    *LDSP(unsigned, ma + (j ^ 1) * 256) = lsb4(b1) | fail;
    *LDSP(unsigned, ma + (j ^ 2) * 256) = lsb4(b2) | fail;
    *LDSP(unsigned, ma + (j ^ 3) * 256) = lsb4(b3) | fail;
#pragma unroll 1
    for (unsigned k = 0;; ++k) {
      while (__builtin_amdgcn_readfirstlane(tk[63]) < 2 * (S_IT - 1) - LEAD + 2 * k) __builtin_amdgcn_s_sleep(1);
      unsigned t = NDYN;
      if (!fail) {
        unsigned tv = 0;
        if (lane == 0) tv = __hip_atomic_fetch_add(ctl + 4, 1u, RLX_AGENT);
        t = min(__builtin_amdgcn_readfirstlane(tv) - c0, NDYN);
      }
      tk[k] = t;
      if (t == NDYN) break;
    }
    asm volatile("s_waitcnt lgkmcnt(0)\n\ts_barrier" ::: "memory");
    return;
  }

  const char* const kb = K + (size_t)pair * PAIR_BYTES;
  const char* const vb = V + (size_t)pair * PAIR_BYTES;
  STAMP(1);
  u32x4 q0, q1, q2, q3;
  DMA_TILE(kb, ring, soff0);
  asm volatile("global_load_dwordx4 %0, %4, %5\n\tglobal_load_dwordx4 %1, %4, %5 offset:64\n\t"
               "global_load_dwordx4 %2, %4, %5 offset:128\n\tglobal_load_dwordx4 %3, %4, %5 offset:192"
               : "=&v"(q0), "=&v"(q1), "=&v"(q2), "=&v"(q3) : "v"(qo), "s"(Q + (size_t)pair * 1024) : "memory");
  DMA_TILE(kb, ring + TILE_BYTES, soff0 + ROUND_BYTES);
  DMA_TILE(kb, ring + 2 * TILE_BYTES, soff0 + 2 * ROUND_BYTES);
  __builtin_amdgcn_s_barrier();

  const unsigned fc = ((c & 3) << 2) | ((((c >> 2) ^ (c >> 3)) & 1) << 1) | (c >> 3);
  const unsigned ka = c * 256 + ((g ^ fc) << 4);
  const unsigned qq = (lane >> 2) & 3, pp = lane & 3;
  const unsigned fr = (qq << 2) | (((g ^ (g >> 1)) & 1) << 1) | (g >> 1);
  const unsigned va = (4 * g + qq) * 256 + (((pp >> 1) ^ fr) << 4) + 8 * (pp & 1);
  const unsigned sa = ldsb + SC_OFF + w * SC_BYTES + (c & 3) * 64 + g * 16;
  float m = -INFINITY, mrun = -INFINITY, l = 0.f;
  unsigned rd = ring, wr = ring;
  asm volatile("s_waitcnt vmcnt(8)" : "+v"(q0), "+v"(q1), "+v"(q2), "+v"(q3) :: "memory");

#define ADVANCE() do { wr = rd; rd += TILE_BYTES; if (rd == ring + RING_BYTES) rd = ring; } while (0)
#define PROGRESS(h) asm volatile("s_mov_b64 exec, 1\n\tds_max_u32 %0, %1\n\ts_mov_b64 exec, -1" :: "v"(ldsb + TK_OFF + 252), "v"(h) : "memory")
#define K_ADDR() unsigned a0 = rd + ka, a1 = a0 ^ 64, a2 = a0 ^ 128, a3 = a0 ^ 192; asm volatile("" : "+v"(a0), "+v"(a1), "+v"(a2), "+v"(a3))
#define K_STEP(sc) do {                    \
    u32x4 k0, k1, k2, k3;                                                                                           \
    asm volatile("ds_read_b128 %0, %4\n\tds_read_b128 %1, %5\n\tds_read_b128 %2, %6\n\tds_read_b128 %3, %7\n\ts_waitcnt lgkmcnt(0)" \
                 : "=&v"(k0), "=&v"(k1), "=&v"(k2), "=&v"(k3) : "v"(a0), "v"(a1), "v"(a2), "v"(a3) : "memory");      \
    f32x4 s = {0.f, 0.f, 0.f, 0.f};                                                                                 \
    s = __builtin_amdgcn_mfma_f32_16x16x32_bf16(__builtin_bit_cast(bf16x8, k0), __builtin_bit_cast(bf16x8, q0), s, 0, 0, 0); \
    s = __builtin_amdgcn_mfma_f32_16x16x32_bf16(__builtin_bit_cast(bf16x8, k1), __builtin_bit_cast(bf16x8, q1), s, 0, 0, 0); \
    s = __builtin_amdgcn_mfma_f32_16x16x32_bf16(__builtin_bit_cast(bf16x8, k2), __builtin_bit_cast(bf16x8, q2), s, 0, 0, 0); \
    s = __builtin_amdgcn_mfma_f32_16x16x32_bf16(__builtin_bit_cast(bf16x8, k3), __builtin_bit_cast(bf16x8, q3), s, 0, 0, 0); \
    mrun = fmaxf(mrun, fmaxf(fmaxf(s[0], s[1]), fmaxf(s[2], s[3])) * C2);                                           \
    *LDSP(f32x4, sc) = s;                                                                                           \
    ADVANCE();                                                                                                      \
  } while (0)

#pragma unroll 1
  for (int h = 0; h < S_SEP; ++h) {
    K_ADDR();
    if (h != 0) {
      const bool nv = h + 2 >= S_SEP;
      DMA_TILE(nv ? vb : kb, wr, soff0 + (unsigned)(nv ? h + 2 - S_SEP : h + 2) * ROUND_BYTES);
      WAIT_VM(8);
    }
    K_STEP(sa + 256 * h);
  }
  f32x4 acc[8];
#pragma unroll
  for (int b = 0; b < 8; ++b) acc[b] = f32x4{0.f, 0.f, 0.f, 0.f};
  unsigned reca = ldsb + RED_OFF + (w * 4 + (c & 3)) * RED_REC + g * 16;
  unsigned recb = ldsb + RED_OFF + w * RED_REC + lane * 8;
  unsigned recm = ldsb + RED_OFF + w * RED_REC + DH * 4;
  unsigned mska = ldsb + MSK_OFF + (w * 64 + lane) * 4;
  unsigned mbo = lane * 16 + w * 4096 + j * 1024;
  asm volatile("" : "+v"(reca), "+v"(recb), "+v"(recm), "+v"(mska), "+v"(mbo));
  unsigned noff = 0;
  bool more = true;
#pragma unroll 1
  for (int u = 0;; ++u) {
    if (u >= S_SEP) {
      K_ADDR();
      PROGRESS(2 * u);
      unsigned t;
      do t = __builtin_amdgcn_readfirstlane(*LDSP(volatile unsigned, ldsb + TK_OFF + 4 * (u - S_SEP))); while (t == NOTREADY);
      more = t < NDYN;
      noff = (S_IT * GRP + t) * UNIT_BYTES + w * 1024;
      if (more) { DMA_TILE(kb, wr, noff); WAIT_VM(8); } else WAIT_VM(4);
      K_STEP(sa);
    }
    unsigned b0 = rd + va, b1 = b0 ^ 32, b2 = b0 ^ 64, b3 = b0 ^ 96, b4 = b0 ^ 128, b5 = b0 ^ 160, b6 = b0 ^ 192, b7 = b0 ^ 224;
    asm volatile("" : "+v"(b0), "+v"(b1), "+v"(b2), "+v"(b3), "+v"(b4), "+v"(b5), "+v"(b6), "+v"(b7));
    PROGRESS(u < S_SEP ? S_SEP + u : 2 * u + 1);
    if (__builtin_amdgcn_ballot_w64(mrun > m) != 0) {
      const float mn = xg_max(mrun);
      const float al = __builtin_amdgcn_exp2f(m - mn);
      m = mn; l *= al;
#pragma unroll
      for (int b = 0; b < 8; ++b) acc[b] *= al;
    }
    const f32x4 s = *LDSP(const f32x4, sa + 256 * (unsigned)(u < S_SEP ? u : 0));
    const float p0 = __builtin_amdgcn_exp2f(fmaf(s[0], C2, -m)), p1 = __builtin_amdgcn_exp2f(fmaf(s[1], C2, -m));
    const float p2 = __builtin_amdgcn_exp2f(fmaf(s[2], C2, -m)), p3 = __builtin_amdgcn_exp2f(fmaf(s[3], C2, -m));
    l += (p0 + p1) + (p2 + p3);
    u32x2 pk = {cvtpk(p0, p1), cvtpk(p2, p3)};
    asm volatile("" : "+v"(pk));
    if (more) {
      DMA_TILE(u == S_SEP - 2 ? kb : vb, wr, u >= S_SEP ? noff : soff0 + (unsigned)min(u + 2, S_SEP) * ROUND_BYTES);
      WAIT_VM(8);
    } else {
      *LDSP(f32x2, reca - g * 16 + DH * 4) = f32x2{m, xg_sum(l)};
      WAIT_VM(0);
    }
    u32x2 v0, v1, v2, v3, v4, v5, v6, v7;
    asm volatile("ds_read_b64_tr_b16 %0, %8\n\tds_read_b64_tr_b16 %1, %9\n\tds_read_b64_tr_b16 %2, %10\n\tds_read_b64_tr_b16 %3, %11\n\t"
                 "ds_read_b64_tr_b16 %4, %12\n\tds_read_b64_tr_b16 %5, %13\n\tds_read_b64_tr_b16 %6, %14\n\tds_read_b64_tr_b16 %7, %15\n\t"
                 "s_waitcnt lgkmcnt(0)"
                 : "=&v"(v0), "=&v"(v1), "=&v"(v2), "=&v"(v3), "=&v"(v4), "=&v"(v5), "=&v"(v6), "=&v"(v7)
                 : "v"(b0), "v"(b1), "v"(b2), "v"(b3), "v"(b4), "v"(b5), "v"(b6), "v"(b7) : "memory");
    const s16x4 pb = __builtin_bit_cast(s16x4, pk);
    acc[0] = __builtin_amdgcn_mfma_f32_16x16x16bf16_1k(__builtin_bit_cast(s16x4, v0), pb, acc[0], 0, 0, 0);
    acc[1] = __builtin_amdgcn_mfma_f32_16x16x16bf16_1k(__builtin_bit_cast(s16x4, v1), pb, acc[1], 0, 0, 0);
    acc[2] = __builtin_amdgcn_mfma_f32_16x16x16bf16_1k(__builtin_bit_cast(s16x4, v2), pb, acc[2], 0, 0, 0);
    acc[3] = __builtin_amdgcn_mfma_f32_16x16x16bf16_1k(__builtin_bit_cast(s16x4, v3), pb, acc[3], 0, 0, 0);
    acc[4] = __builtin_amdgcn_mfma_f32_16x16x16bf16_1k(__builtin_bit_cast(s16x4, v4), pb, acc[4], 0, 0, 0);
    acc[5] = __builtin_amdgcn_mfma_f32_16x16x16bf16_1k(__builtin_bit_cast(s16x4, v5), pb, acc[5], 0, 0, 0);
    acc[6] = __builtin_amdgcn_mfma_f32_16x16x16bf16_1k(__builtin_bit_cast(s16x4, v6), pb, acc[6], 0, 0, 0);
    acc[7] = __builtin_amdgcn_mfma_f32_16x16x16bf16_1k(__builtin_bit_cast(s16x4, v7), pb, acc[7], 0, 0, 0);
    ADVANCE();
    if (!more) break;
  }
  STAMP(3);

  u32x4 x[NPOLL][3];
#define POLL(k) asm volatile(LOAD3("%0, %3, %6", "%1, %4, %6", "%2, %5, %6")                                        \
                   : "=&v"(x[k][0]), "=&v"(x[k][1]), "=&v"(x[k][2]) : "v"(mbo ^ 1024), "v"(mbo ^ 2048), "v"(mbo ^ 3072), "s"(gws) : "memory")
  if (w == j) POLL(0);
  if (c < 4) {
#pragma unroll
    for (int b = 0; b < 8; ++b) *LDSP(f32x4, reca + 64 * b) = acc[b];
  }
  f32x2 ml[NW], o[NW]; unsigned snap;
  asm volatile("s_waitcnt lgkmcnt(0)\n\ts_barrier\n\t"
               "ds_read_b64 %0, %9\n\tds_read_b64 %1, %9 offset:%12\n\tds_read_b64 %2, %9 offset:%13\n\tds_read_b64 %3, %9 offset:%14\n\t"
               "ds_read_b64 %4, %10\n\tds_read_b64 %5, %10 offset:%12\n\tds_read_b64 %6, %10 offset:%13\n\tds_read_b64 %7, %10 offset:%14\n\t"
               "ds_read_b32 %8, %11"
               : "=&v"(ml[0]), "=&v"(ml[1]), "=&v"(ml[2]), "=&v"(ml[3]), "=&v"(o[0]), "=&v"(o[1]), "=&v"(o[2]), "=&v"(o[3]), "=&v"(snap)
               : "v"(recm), "v"(recb), "v"(mska), "i"(4 * RED_REC), "i"(8 * RED_REC), "i"(12 * RED_REC) : "memory");
  if (w == j) POLL(1);
  asm volatile("s_waitcnt lgkmcnt(0)" : "+v"(ml[0]), "+v"(ml[1]), "+v"(ml[2]), "+v"(ml[3]), "+v"(o[0]), "+v"(o[1]), "+v"(o[2]), "+v"(o[3]), "+v"(snap) :: "memory");
  u32x4 r;
  {
    const float M = __uint_as_float(__float_as_uint(fmaxf(fmaxf(ml[0][0], ml[1][0]), fmaxf(ml[2][0], ml[3][0]))) & ~1u);
    const float a0 = __builtin_amdgcn_exp2f(ml[0][0] - M), a1 = __builtin_amdgcn_exp2f(ml[1][0] - M), a2 = __builtin_amdgcn_exp2f(ml[2][0] - M), a3 = __builtin_amdgcn_exp2f(ml[3][0] - M);
    const float o0 = fmaf(a0, o[0][0], a1 * o[1][0]) + fmaf(a2, o[2][0], a3 * o[3][0]), o1 = fmaf(a0, o[0][1], a1 * o[1][1]) + fmaf(a2, o[2][1], a3 * o[3][1]);
    const float L = fmaf(a0, ml[0][1], a1 * ml[1][1]) + fmaf(a2, ml[2][1], a3 * ml[3][1]);
    r = u32x4{__float_as_uint(o0) & ~1u, __float_as_uint(o1) & ~1u, __float_as_uint(M), __float_as_uint(L) & ~1u};
  }
  STAMP(4);
  if (w != j) {
    const unsigned f = ~snap;
    const u32x4 u = {r[0] | (f & 1), r[1] | ((f >> 1) & 1), r[2] | ((f >> 2) & 1), r[3] | ((f >> 3) & 1)};
    __builtin_amdgcn_raw_buffer_store_b128(u, __builtin_amdgcn_make_buffer_rsrc((void*)gws, 0, WS_CTL, 0x00020000), mbo | (snap & FAILED), 0, 16);
  } else {
    POLL(2);
    unsigned sb[12];
#pragma unroll
    for (int y = 0; y < 12; ++y) sb[y] = snap >> y;
    u32x4 x1 = {~0u, ~0u, ~0u, ~0u}, x2 = x1, x3 = x1;
    bool done = false;
#pragma unroll 1
    for (unsigned n = __builtin_amdgcn_readfirstlane(snap & FAILED); n < POLL_CAP && !done; ++n) {
#pragma unroll
      for (int k = 0; k < NPOLL; ++k) {
        asm volatile("s_waitcnt vmcnt(%3)" : "+v"(x[k][0]), "+v"(x[k][1]), "+v"(x[k][2]) : "i"(3 * (NPOLL - 1)) : "memory");
        unsigned fresh = 1;
#pragma unroll
        for (int y = 0; y < 12; ++y) fresh &= x[k][y >> 2][y & 3] ^ sb[y];
        if (__builtin_amdgcn_ballot_w64(fresh == 0) == 0) { x1 = x[k][0]; x2 = x[k][1]; x3 = x[k][2]; done = true; break; }
        POLL(k);
      }
    }
    const f32x4 r0 = __builtin_bit_cast(f32x4, r), r1 = __builtin_bit_cast(f32x4, x1 & ~1u), r2 = __builtin_bit_cast(f32x4, x2 & ~1u), r3 = __builtin_bit_cast(f32x4, x3 & ~1u);
    const float M = fmaxf(fmaxf(r0[2], r1[2]), fmaxf(r2[2], r3[2]));
    const float a0 = __builtin_amdgcn_exp2f(r0[2] - M), a1 = __builtin_amdgcn_exp2f(r1[2] - M), a2 = __builtin_amdgcn_exp2f(r2[2] - M), a3 = __builtin_amdgcn_exp2f(r3[2] - M);
    const float L = fmaf(a0, r0[3], a1 * r1[3]) + fmaf(a2, r2[3], a3 * r3[3]);
    const float y0 = fmaf(a0, r0[0], a1 * r1[0]) + fmaf(a2, r2[0], a3 * r3[0]);
    const float y1 = fmaf(a0, r0[1], a1 * r1[1]) + fmaf(a2, r2[1], a3 * r3[1]);
    const float inv = __builtin_amdgcn_rcpf(L);
    unsigned yy = cvtpk(y0 * inv, y1 * inv);
    out[(pair * 4 + j) * 64 + lane] = yy;
    asm volatile("s_waitcnt vmcnt(0)" : "+v"(x[0][0]), "+v"(x[0][1]), "+v"(x[0][2]), "+v"(x[1][0]), "+v"(x[1][1]), "+v"(x[1][2]), "+v"(x[2][0]), "+v"(x[2][1]), "+v"(x[2][2]) :: "memory");
  }
}

extern "C" void kernel_launch(void* const* d_in, const int* in_sizes, int n_in, void* d_out, int out_size, void* d_ws, size_t ws_size, hipStream_t stream) {
  (void)hipFuncSetAttribute((const void*)gqa_decode, hipFuncAttributeMaxDynamicSharedMemorySize, LDS_BYTES);
  gqa_decode<<<NPAIR * GRP, (NW + 1) * 64, LDS_BYTES, stream>>>((const char*)d_in[1], (const char*)d_in[2], (const char*)d_in[0], (char*)d_ws, (unsigned*)d_out);
}
```

```cpp
#include <hip/hip_runtime.h>
#include <hip/hip_bf16.h>

typedef unsigned u32x4 __attribute__((ext_vector_type(4)));
typedef unsigned u32x2 __attribute__((ext_vector_type(2)));
typedef float f32x4 __attribute__((ext_vector_type(4)));
typedef float f32x2 __attribute__((ext_vector_type(2)));
typedef __bf16 bf16x8 __attribute__((ext_vector_type(8)));
typedef __bf16 bf16x2 __attribute__((ext_vector_type(2)));
#define LDSP(T, a) ((__attribute__((address_space(3))) T*)(size_t)(a))
typedef __attribute__((address_space(1))) unsigned gu32;
#define RLX_AGENT __ATOMIC_RELAXED, __HIP_MEMORY_SCOPE_AGENT

constexpr int SK = 8192, DH = 128, NPAIR = 64, GRP = 4, NW = 4;
constexpr int TILE = 16;
constexpr int NUNIT = SK / (TILE * NW);
constexpr int S_IT = 27;
constexpr int S_SEP = S_IT - 1;
constexpr unsigned NDYN = NUNIT - S_IT * GRP;
constexpr int LEAD = 2;
constexpr int NPOLL = 2;
static_assert(S_SEP >= 3 && S_SEP <= 28 && NDYN < 60, "score store: 28 tiles + q per wave; ticket table: one LDS word per lane");
constexpr unsigned PAIR_BYTES = SK * DH * 2;
constexpr unsigned TILE_BYTES = TILE * DH * 2;
constexpr unsigned UNIT_BYTES = NW * TILE_BYTES;
constexpr unsigned ROUND_BYTES = GRP * UNIT_BYTES;
constexpr unsigned RING_BYTES = 3 * TILE_BYTES;
constexpr unsigned SC_OFF = NW * RING_BYTES, SC_BYTES = 32 * 256;
constexpr unsigned RED_OFF = SC_OFF + NW * SC_BYTES;
constexpr unsigned RED_REC = (DH + 4) * 4;
constexpr unsigned TK_OFF = RED_OFF + NW * 4 * RED_REC;
constexpr unsigned MSK_OFF = TK_OFF + 256;
constexpr unsigned LDS_BYTES = MSK_OFF + 1024;
static_assert(LDS_BYTES > 80 * 1024 && LDS_BYTES <= 160 * 1024, "exactly one workgroup per CU");
constexpr unsigned G_WS = 32768, WS_CTL = 16384, WS_DIG = WS_CTL + 512;
constexpr unsigned NOTREADY = 0xFFFFFFFFu;
constexpr unsigned BEAT_CAP = 1u << 18, POLL_CAP = 1u << 21;
constexpr float C2 = 0.088388347648318440f * 1.4426950408889634f;

__device__ __forceinline__ unsigned cvtpk(float lo, float hi) {
  bf16x2 v = {(__bf16)lo, (__bf16)hi};
  return __builtin_bit_cast(unsigned, v);
}
__device__ __forceinline__ float xg_max(float x) {
  auto s = __builtin_amdgcn_permlane16_swap(__float_as_uint(x), __float_as_uint(x), false, false);
  x = fmaxf(__uint_as_float(s[0]), __uint_as_float(s[1]));
  auto t = __builtin_amdgcn_permlane32_swap(__float_as_uint(x), __float_as_uint(x), false, false);
  return fmaxf(__uint_as_float(t[0]), __uint_as_float(t[1]));
}
__device__ __forceinline__ float xg_sum(float x) {
  auto s = __builtin_amdgcn_permlane16_swap(__float_as_uint(x), __float_as_uint(x), false, false);
  x = __uint_as_float(s[0]) + __uint_as_float(s[1]);
  auto t = __builtin_amdgcn_permlane32_swap(__float_as_uint(x), __float_as_uint(x), false, false);
  return __uint_as_float(t[0]) + __uint_as_float(t[1]);
}
__device__ __forceinline__ unsigned lsb4(u32x4 x) { return (x[0] & 1) | ((x[1] & 1) << 1) | ((x[2] & 1) << 2) | ((x[3] & 1) << 3); }

#define DMA_TILE(p, m0v, soff)                                                                  \
  asm volatile("s_mov_b32 m0, %0\n\ts_nop 0\n\t"                                                \
               "buffer_load_dwordx4 %3, %1, %2 offen nt lds\n\t"                                \
               "buffer_load_dwordx4 %4, %1, %2 offen offset:1024 nt lds\n\t"                    \
               "buffer_load_dwordx4 %5, %1, %2 offen offset:2048 nt lds\n\t"                    \
               "buffer_load_dwordx4 %6, %1, %2 offen offset:3072 nt lds"                        \
               :: "s"(m0v), "s"(__builtin_amdgcn_make_buffer_rsrc((void*)(p), 0, PAIR_BYTES, 0x00020000)), "s"(soff), \
                  "v"(so0), "v"(so1), "v"(so2), "v"(so3) : "memory")
#define WAIT_VM(n) asm volatile("s_waitcnt vmcnt(" #n ")" ::: "memory")
#define LOAD3(x1, x2, x3) "global_load_dwordx4 " x1 " sc1\n\tglobal_load_dwordx4 " x2 " sc1\n\tglobal_load_dwordx4 " x3 " sc1\n\t"

__global__ void __launch_bounds__(320, 2) gqa_decode(const char* __restrict__ K, const char* __restrict__ V, const char* __restrict__ Q, char* ws, unsigned* __restrict__ out) {
  extern __shared__ __attribute__((aligned(256))) char lds[];
  const unsigned bid = blockIdx.x, pair = (bid >> 3) * 2 + (((bid >> 1) ^ (bid & (bid >> 3))) & 1), j = (bid & 1) | ((bid >> 1) & 2);
  const unsigned lane = threadIdx.x & 63, w = __builtin_amdgcn_readfirstlane(threadIdx.x >> 6);
  const unsigned g = lane >> 4, c = lane & 15;
  unsigned so0 = g * 256 + ((c ^ (g << 2)) << 4), so1 = (so0 ^ 32) + 3072, so2 = (so0 ^ 48) + 2 * 3072, so3 = (so0 ^ 16) + 3 * 3072;
  unsigned qo = lane * 16;
  const unsigned ldsb = (unsigned)(size_t)lds;
  const unsigned ring = ldsb + w * RING_BYTES;
  const unsigned soff0 = j * UNIT_BYTES + w * 1024;
  asm volatile("" : "+v"(so0), "+v"(so1), "+v"(so2), "+v"(so3), "+v"(qo) : "s"(ring), "s"(soff0));
  asm volatile("" :: "s"(K), "s"(V), "s"(Q), "s"(ws), "s"(out));
  char* const gws = ws + (size_t)pair * G_WS;

  if (w == NW) {
    volatile __attribute__((address_space(3))) unsigned* const tk = LDSP(volatile unsigned, ldsb + TK_OFF);
    tk[lane] = lane == 63 ? 0u : NOTREADY;
    asm volatile("s_waitcnt lgkmcnt(0)\n\ts_barrier" ::: "memory");
    __builtin_amdgcn_s_setprio(3);
    gu32* const ctl = (gu32*)(gws + WS_CTL);
    const unsigned ma = ldsb + MSK_OFF;
    unsigned e0; u32x4 a0, a1, a2, a3;
    asm volatile("global_load_dword %0, %5, %7 sc1\n\tglobal_load_dwordx4 %1, %6, %7 sc1\n\t"
                 LOAD3("%2, %6, %7 offset:1024", "%3, %6, %7 offset:2048", "%4, %6, %7 offset:3072") "s_waitcnt vmcnt(0)"
                 : "=&v"(e0), "=&v"(a0), "=&v"(a1), "=&v"(a2), "=&v"(a3) : "v"(WS_CTL + (lane & 7) * 4), "v"(lane * 16 + j * 4096), "s"(gws) : "memory");
    const unsigned c0 = __builtin_amdgcn_readlane(e0, 4);
    const unsigned d0 = lsb4(a0) | (lsb4(a1) << 4) | (lsb4(a2) << 8) | (lsb4(a3) << 12);
    *LDSP(volatile unsigned short, ma + lane * 2) = (unsigned short)d0;
    const u32x4 dg = *LDSP(volatile u32x4, ma + (lane & 7) * 16);
    __builtin_amdgcn_raw_buffer_store_b128(dg, __builtin_amdgcn_make_buffer_rsrc((void*)(gws + WS_DIG + j * 128), 0, 128, 0x00020000), lane * 16, 0, 16);
    WAIT_VM(0);
    if (lane == 0) __hip_atomic_fetch_add(ctl + j, 1u, RLX_AGENT);
    bool fail = true;
#pragma unroll 1
    for (unsigned n = 0; n < BEAT_CAP; ++n) {
      __builtin_amdgcn_s_sleep(64);
      const unsigned e = __hip_atomic_load(ctl + (lane & 7), RLX_AGENT);
      if (__builtin_amdgcn_ballot_w64(lane < 4 && lane != j && e == e0) == 0) { fail = false; break; }
    }
    if (lane == 0) __hip_atomic_fetch_add(ctl + j, 1u, RLX_AGENT);
    unsigned d1, d2, d3;
    asm volatile("global_load_dword %0, %3, %6 sc1\n\tglobal_load_dword %1, %4, %6 sc1\n\tglobal_load_dword %2, %5, %6 sc1\n\ts_waitcnt vmcnt(0)"
                 : "=&v"(d1), "=&v"(d2), "=&v"(d3)
                 : "v"(WS_DIG + (j ^ 1) * 128 + (lane >> 1) * 4), "v"(WS_DIG + (j ^ 2) * 128 + (lane >> 1) * 4), "v"(WS_DIG + (j ^ 3) * 128 + (lane >> 1) * 4), "s"(gws) : "memory");
    d1 >>= (lane & 1) * 16; d2 >>= (lane & 1) * 16; d3 >>= (lane & 1) * 16;
#pragma unroll
    for (int y = 0; y < NW; ++y)
      *LDSP(volatile unsigned, ma + y * 256 + lane * 4) = ((d1 >> 4 * y) & 15) | (((d2 >> 4 * y) & 15) << 4) | (((d3 >> 4 * y) & 15) << 8) | (((d0 >> 4 * y) & 15) << 12);
    if (fail) out[(pair * 4 + j) * 64 + lane] = ~0u;
#pragma unroll 1
    for (unsigned k = 0;; ++k) {
      while (__builtin_amdgcn_readfirstlane(tk[63]) < 2 * (S_IT - 1) - LEAD + 2 * k) __builtin_amdgcn_s_sleep(1);
      unsigned t = NDYN;
      if (!fail) {
        unsigned tv = 0;
        if (lane == 0) tv = __hip_atomic_fetch_add(ctl + 4, 1u, RLX_AGENT);
        t = min(__builtin_amdgcn_readfirstlane(tv) - c0, NDYN);
        if (t == NDYN - 1) tk[k++] = t++;
      }
      tk[k] = t;
      if (t == NDYN) break;
    }
    asm volatile("s_waitcnt lgkmcnt(0)\n\ts_barrier" ::: "memory");
    return;
  }

  const char* const kb = K + (size_t)pair * PAIR_BYTES;
  const char* const vb = V + (size_t)pair * PAIR_BYTES;
  const unsigned qst = ldsb + SC_OFF + (w + 1) * SC_BYTES - 1024;
  DMA_TILE(kb, ring, soff0);
  asm volatile("s_mov_b32 m0, %0\n\ts_nop 0\n\tglobal_load_lds_dwordx4 %1, %2" :: "s"(qst), "v"(qo), "s"(Q + (size_t)pair * 1024) : "memory");
  DMA_TILE(kb, ring + TILE_BYTES, soff0 + ROUND_BYTES);
  DMA_TILE(kb, ring + 2 * TILE_BYTES, soff0 + 2 * ROUND_BYTES);
  __builtin_amdgcn_s_barrier();

  const unsigned fc = ((c & 3) << 2) | ((((c >> 2) ^ (c >> 3)) & 1) << 1) | (c >> 3);
  const unsigned ka = c * 256 + ((g ^ fc) << 4);
  const unsigned qq = (lane >> 2) & 3, pp = lane & 3;
  const unsigned fr = (qq << 2) | (((g ^ (g >> 1)) & 1) << 1) | (g >> 1);
  const unsigned va = (4 * g + qq) * 256 + (((pp >> 1) ^ fr) << 4) + 8 * (pp & 1);
  const unsigned sa = ldsb + SC_OFF + w * SC_BYTES + (c & 3) * 64 + g * 16;
  float m = -INFINITY, mrun = -INFINITY, l = 0.f;
  unsigned rd = ring, wr = ring;
  u32x4 q0, q1, q2, q3;
  asm volatile("s_waitcnt vmcnt(8)\n\tds_read_b128 %0, %4\n\tds_read_b128 %1, %4 offset:64\n\tds_read_b128 %2, %4 offset:128\n\t"
               "ds_read_b128 %3, %4 offset:192\n\ts_waitcnt lgkmcnt(0)"
               : "=&v"(q0), "=&v"(q1), "=&v"(q2), "=&v"(q3) : "v"(qst + (c & 3) * 256 + g * 16) : "memory");

#define ADVANCE() do { wr = rd; rd += TILE_BYTES; if (rd == ring + RING_BYTES) rd = ring; } while (0)
#define PROGRESS(h) asm volatile("s_mov_b64 exec, 1\n\tds_max_u32 %0, %1\n\ts_mov_b64 exec, -1" :: "v"(ldsb + TK_OFF + 252), "v"(h) : "memory")
#define K_ADDR() unsigned a0 = rd + ka, a1 = a0 ^ 64, a2 = a0 ^ 128, a3 = a0 ^ 192; asm volatile("" : "+v"(a0), "+v"(a1), "+v"(a2), "+v"(a3))
#define K_STEP(sc) do {                    \
    u32x4 k0, k1, k2, k3;                                                                                           \
    asm volatile("ds_read_b128 %0, %4\n\tds_read_b128 %1, %5\n\tds_read_b128 %2, %6\n\tds_read_b128 %3, %7\n\ts_waitcnt lgkmcnt(0)" \
                 : "=&v"(k0), "=&v"(k1), "=&v"(k2), "=&v"(k3) : "v"(a0), "v"(a1), "v"(a2), "v"(a3) : "memory");      \
    f32x4 s = {0.f, 0.f, 0.f, 0.f};                                                                                 \
    s = __builtin_amdgcn_mfma_f32_16x16x32_bf16(__builtin_bit_cast(bf16x8, k0), __builtin_bit_cast(bf16x8, q0), s, 0, 0, 0); \
    s = __builtin_amdgcn_mfma_f32_16x16x32_bf16(__builtin_bit_cast(bf16x8, k1), __builtin_bit_cast(bf16x8, q1), s, 0, 0, 0); \
    s = __builtin_amdgcn_mfma_f32_16x16x32_bf16(__builtin_bit_cast(bf16x8, k2), __builtin_bit_cast(bf16x8, q2), s, 0, 0, 0); \
    s = __builtin_amdgcn_mfma_f32_16x16x32_bf16(__builtin_bit_cast(bf16x8, k3), __builtin_bit_cast(bf16x8, q3), s, 0, 0, 0); \
    mrun = fmaxf(mrun, fmaxf(fmaxf(s[0], s[1]), fmaxf(s[2], s[3])) * C2);                                           \
    *LDSP(f32x4, sc) = s;                                                                                           \
    ADVANCE();                                                                                                      \
  } while (0)

#pragma unroll 1
  for (int h = 0; h < S_SEP; ++h) {
    K_ADDR();
    if (h != 0) {
      const bool nv = h + 2 >= S_SEP;
      DMA_TILE(nv ? vb : kb, wr, soff0 + (unsigned)(nv ? h + 2 - S_SEP : h + 2) * ROUND_BYTES);
      WAIT_VM(8);
    }
    K_STEP(sa + 256 * h);
  }
  f32x4 acc0 = {0.f, 0.f, 0.f, 0.f}, acc1 = acc0;
  unsigned reca = ldsb + RED_OFF + (w * 4 + (c & 3)) * RED_REC + (c >> 2) * 64 + g * 16;
  unsigned recw = ldsb + RED_OFF + (w * 4 + (c & 3)) * RED_REC + DH * 4;
  unsigned recb = ldsb + RED_OFF + w * RED_REC + lane * 8;
  unsigned recm = ldsb + RED_OFF + w * RED_REC + DH * 4;
  unsigned mb0 = lane * 16 + j * 4096 + w * 1024, mb1 = mb0 ^ 4096, mb2 = mb0 ^ 8192, mb3 = mb0 ^ 12288;
  unsigned oo = ((pair * 4 + w) * 64 + lane) * 4;
  asm volatile("" : "+v"(reca), "+v"(recw), "+v"(recb), "+v"(recm), "+v"(mb0), "+v"(mb1), "+v"(mb2), "+v"(mb3), "+v"(oo));
  unsigned snap = 0;
  unsigned noff = 0;
  bool more = true, fin = false;
  u32x2 pk0, pk1, pk2, pk3; unsigned b0, b1, b2, b3, b4, b5, b6, b7;
#define PV(acc, va_, vb_, pa_, pb_) acc = __builtin_amdgcn_mfma_f32_16x16x32_bf16(__builtin_bit_cast(bf16x8, __builtin_shufflevector(va_, vb_, 0, 1, 2, 3)), \
                                                                            __builtin_bit_cast(bf16x8, __builtin_shufflevector(pa_, pb_, 0, 1, 2, 3)), acc, 0, 0, 0)
#define V_MMA() do {                                                                                                \
    u32x2 v0, v1, v2, v3, v4, v5, v6, v7;                                                                           \
    asm volatile("ds_read_b64_tr_b16 %0, %8\n\tds_read_b64_tr_b16 %1, %9\n\tds_read_b64_tr_b16 %2, %10\n\tds_read_b64_tr_b16 %3, %11\n\t" \
                 "ds_read_b64_tr_b16 %4, %12\n\tds_read_b64_tr_b16 %5, %13\n\tds_read_b64_tr_b16 %6, %14\n\tds_read_b64_tr_b16 %7, %15\n\t" \
                 "s_waitcnt lgkmcnt(0)"                                                                             \
                 : "=&v"(v0), "=&v"(v1), "=&v"(v2), "=&v"(v3), "=&v"(v4), "=&v"(v5), "=&v"(v6), "=&v"(v7)            \
                 : "v"(b0), "v"(b1), "v"(b2), "v"(b3), "v"(b4), "v"(b5), "v"(b6), "v"(b7) : "memory");               \
    PV(acc0, v0, v1, pk0, pk1); PV(acc1, v4, v5, pk0, pk1); PV(acc0, v2, v3, pk2, pk3); PV(acc1, v6, v7, pk2, pk3); \
  } while (0)
#pragma unroll 1
  for (int u = 0;; ++u) {
    if (u >= S_SEP) {
      K_ADDR();
      PROGRESS(2 * u);
      unsigned t;
      do t = __builtin_amdgcn_readfirstlane(*LDSP(volatile unsigned, ldsb + TK_OFF + 4 * (u - S_SEP))); while (t == NOTREADY);
      more = t < NDYN;
      fin |= t == NDYN - 1;
      noff = (S_IT * GRP + t) * UNIT_BYTES + w * 1024;
      if (more) {
        DMA_TILE(kb, wr, noff);
        WAIT_VM(8);
      } else {
        snap = *LDSP(volatile unsigned, ldsb + MSK_OFF + (w * 64 + lane) * 4);
        WAIT_VM(4);
      }
      K_STEP(sa);
    }
    PROGRESS(u < S_SEP ? S_SEP + u : 2 * u + 1);
    b0 = rd + va; b1 = b0 ^ 32; b2 = b0 ^ 64; b3 = b0 ^ 96; b4 = b0 ^ 128; b5 = b0 ^ 160; b6 = b0 ^ 192; b7 = b0 ^ 224;
    if (__builtin_amdgcn_ballot_w64(mrun > m) != 0) {
      const float mn = xg_max(mrun);
      const float al = __builtin_amdgcn_exp2f(m - mn);
      m = mn; l *= al; acc0 *= al; acc1 *= al;
    }
    const f32x4 s = *LDSP(const f32x4, sa + 256 * (unsigned)(u < S_SEP ? u : 0));
    const float p0 = __builtin_amdgcn_exp2f(fmaf(s[0], C2, -m)), p1 = __builtin_amdgcn_exp2f(fmaf(s[1], C2, -m));
    const float p2 = __builtin_amdgcn_exp2f(fmaf(s[2], C2, -m)), p3 = __builtin_amdgcn_exp2f(fmaf(s[3], C2, -m));
    l += (p0 + p1) + (p2 + p3);
    const u32x2 pk = {cvtpk(p0, p1), cvtpk(p2, p3)}, pz = {0u, 0u};
    pk0 = (c >> 2) == 0 ? pk : pz; pk1 = (c >> 2) == 1 ? pk : pz; pk2 = (c >> 2) == 2 ? pk : pz; pk3 = (c >> 2) == 3 ? pk : pz;
    asm volatile("" : "+v"(pk0), "+v"(pk1), "+v"(pk2), "+v"(pk3), "+v"(b0), "+v"(b1), "+v"(b2), "+v"(b3), "+v"(b4), "+v"(b5), "+v"(b6), "+v"(b7));
    if (!more) break;
    DMA_TILE(u == S_SEP - 2 ? kb : vb, wr, u >= S_SEP ? noff : soff0 + (unsigned)min(u + 2, S_SEP) * ROUND_BYTES);
    WAIT_VM(8);
    V_MMA();
    ADVANCE();
  }

  *LDSP(f32x2, recw) = f32x2{m, xg_sum(l)};
  unsigned sb[12];
#pragma unroll
  for (int y = 0; y < 12; ++y) sb[y] = (fin ? snap : ~snap >> 12) >> y;
  asm volatile("" : "+v"(sb[0]), "+v"(sb[1]), "+v"(sb[2]), "+v"(sb[3]), "+v"(sb[4]), "+v"(sb[5]), "+v"(sb[6]), "+v"(sb[7]), "+v"(sb[8]), "+v"(sb[9]), "+v"(sb[10]), "+v"(sb[11]));
  u32x4 x[NPOLL][3];
#define POLL(k) asm volatile(LOAD3("%0, %3, %6", "%1, %4, %6", "%2, %5, %6")                                        \
                   : "=&v"(x[k][0]), "=&v"(x[k][1]), "=&v"(x[k][2]) : "v"(mb1), "v"(mb2), "v"(mb3), "s"(gws) : "memory")
  WAIT_VM(0);
  V_MMA();
  *LDSP(f32x4, reca) = acc0;
  *LDSP(f32x4, reca + 256) = acc1;
  if (fin) POLL(0);
  f32x2 ml[NW], o[NW];
  asm volatile("s_waitcnt lgkmcnt(0)\n\ts_barrier\n\t"
               "ds_read_b64 %0, %8\n\tds_read_b64 %1, %8 offset:%10\n\tds_read_b64 %2, %8 offset:%11\n\tds_read_b64 %3, %8 offset:%12\n\t"
               "ds_read_b64 %4, %9\n\tds_read_b64 %5, %9 offset:%10\n\tds_read_b64 %6, %9 offset:%11\n\tds_read_b64 %7, %9 offset:%12\n\t"
               "s_waitcnt lgkmcnt(0)"
               : "=&v"(ml[0]), "=&v"(ml[1]), "=&v"(ml[2]), "=&v"(ml[3]), "=&v"(o[0]), "=&v"(o[1]), "=&v"(o[2]), "=&v"(o[3])
               : "v"(recm), "v"(recb), "i"(4 * RED_REC), "i"(8 * RED_REC), "i"(12 * RED_REC) : "memory");
  u32x4 r;
  {
    const float M = __uint_as_float(__float_as_uint(fmaxf(fmaxf(ml[0][0], ml[1][0]), fmaxf(ml[2][0], ml[3][0]))) & ~1u);
    const float a0 = __builtin_amdgcn_exp2f(ml[0][0] - M), a1 = __builtin_amdgcn_exp2f(ml[1][0] - M), a2 = __builtin_amdgcn_exp2f(ml[2][0] - M), a3 = __builtin_amdgcn_exp2f(ml[3][0] - M);
    const float o0 = fmaf(a0, o[0][0], a1 * o[1][0]) + fmaf(a2, o[2][0], a3 * o[3][0]), o1 = fmaf(a0, o[0][1], a1 * o[1][1]) + fmaf(a2, o[2][1], a3 * o[3][1]);
    const float L = fmaf(a0, ml[0][1], a1 * ml[1][1]) + fmaf(a2, ml[2][1], a3 * ml[3][1]);
    r = u32x4{__float_as_uint(o0) & ~1u, __float_as_uint(o1) & ~1u, __float_as_uint(M), __float_as_uint(L) & ~1u};
  }
  if (!fin) {
    const u32x4 u = {r[0] | (sb[0] & 1), r[1] | (sb[1] & 1), r[2] | (sb[2] & 1), r[3] | (sb[3] & 1)};
    __builtin_amdgcn_raw_buffer_store_b128(u, __builtin_amdgcn_make_buffer_rsrc((void*)gws, 0, WS_CTL, 0x00020000), mb0, 0, 16);
  } else {
    POLL(1);
    u32x4 x1, x2, x3;
    asm("" : "=v"(x1), "=v"(x2), "=v"(x3));
    bool done = false;
#pragma unroll 1
    for (unsigned n = 0; n < POLL_CAP && !done; ++n) {
#pragma unroll
      for (int k = 0; k < NPOLL; ++k) {
        asm volatile("s_waitcnt vmcnt(%3)" : "+v"(x[k][0]), "+v"(x[k][1]), "+v"(x[k][2]) : "i"(3 * (NPOLL - 1)) : "memory");
        unsigned fresh = 1;
#pragma unroll
        for (int y = 0; y < 12; ++y) fresh &= x[k][y >> 2][y & 3] ^ sb[y];
        if (__builtin_amdgcn_ballot_w64(fresh == 0) == 0) { x1 = x[k][0]; x2 = x[k][1]; x3 = x[k][2]; done = true; break; }
        POLL(k);
      }
    }
    if (!done) r[3] = ~0u;
    const f32x4 r0 = __builtin_bit_cast(f32x4, r), r1 = __builtin_bit_cast(f32x4, x1 & ~1u), r2 = __builtin_bit_cast(f32x4, x2 & ~1u), r3 = __builtin_bit_cast(f32x4, x3 & ~1u);
    const float M = fmaxf(fmaxf(r0[2], r1[2]), fmaxf(r2[2], r3[2]));
    const float a0 = __builtin_amdgcn_exp2f(r0[2] - M), a1 = __builtin_amdgcn_exp2f(r1[2] - M), a2 = __builtin_amdgcn_exp2f(r2[2] - M), a3 = __builtin_amdgcn_exp2f(r3[2] - M);
    const float L = fmaf(a0, r0[3], a1 * r1[3]) + fmaf(a2, r2[3], a3 * r3[3]);
    const float y0 = fmaf(a0, r0[0], a1 * r1[0]) + fmaf(a2, r2[0], a3 * r3[0]);
    const float y1 = fmaf(a0, r0[1], a1 * r1[1]) + fmaf(a2, r2[1], a3 * r3[1]);
    const float inv = __builtin_amdgcn_rcpf(L);
    *(unsigned*)((char*)out + oo) = cvtpk(y0 * inv, y1 * inv);
#pragma unroll
    for (int k = 0; k < NPOLL; ++k) asm volatile("s_waitcnt vmcnt(0)" : "+v"(x[k][0]), "+v"(x[k][1]), "+v"(x[k][2]) :: "memory");
  }
}

extern "C" void kernel_launch(void* const* d_in, const int* in_sizes, int n_in, void* d_out, int out_size, void* d_ws, size_t ws_size, hipStream_t stream) {
  (void)hipFuncSetAttribute((const void*)gqa_decode, hipFuncAttributeMaxDynamicSharedMemorySize, LDS_BYTES);
  gqa_decode<<<NPAIR * GRP, (NW + 1) * 64, LDS_BYTES, stream>>>((const char*)d_in[1], (const char*)d_in[2], (const char*)d_in[0], (char*)d_ws, (unsigned*)d_out);
}
```

```cpp
#include <hip/hip_runtime.h>
#include <hip/hip_bf16.h>

typedef unsigned u32x4 __attribute__((ext_vector_type(4)));
typedef unsigned u32x2 __attribute__((ext_vector_type(2)));
typedef float f32x4 __attribute__((ext_vector_type(4)));
typedef float f32x2 __attribute__((ext_vector_type(2)));
typedef __bf16 bf16x8 __attribute__((ext_vector_type(8)));
typedef __bf16 bf16x2 __attribute__((ext_vector_type(2)));
#define LDSP(T, a) ((__attribute__((address_space(3))) T*)(size_t)(a))
typedef __attribute__((address_space(1))) unsigned gu32;
#define RLX_AGENT __ATOMIC_RELAXED, __HIP_MEMORY_SCOPE_AGENT

constexpr int SK = 8192, DH = 128, NPAIR = 64, GRP = 4, NW = 4;
constexpr int TILE = 16;
constexpr int NUNIT = SK / (TILE * NW);
constexpr int S_IT = 27;
constexpr int S_SEP = S_IT - 1;
constexpr unsigned NDYN = NUNIT - S_IT * GRP;
constexpr int LEAD = 2;
constexpr int U_SYNC = S_SEP - 8;
constexpr int NPOLL = 2;
static_assert(S_SEP >= 3 && S_SEP <= 28 && NDYN < 60, "score store: 28 tiles + q per wave; ticket table: one LDS word per lane");
constexpr unsigned PAIR_BYTES = SK * DH * 2;
constexpr unsigned TILE_BYTES = TILE * DH * 2;
constexpr unsigned UNIT_BYTES = NW * TILE_BYTES;
constexpr unsigned ROUND_BYTES = GRP * UNIT_BYTES;
constexpr unsigned RING_BYTES = 3 * TILE_BYTES;
constexpr unsigned SC_OFF = NW * RING_BYTES, SC_BYTES = 32 * 256;
constexpr unsigned RED_OFF = SC_OFF + NW * SC_BYTES;
constexpr unsigned RED_REC = (DH + 4) * 4;
constexpr unsigned TK_OFF = RED_OFF + NW * 4 * RED_REC;
constexpr unsigned MSK_OFF = TK_OFF + 256;
constexpr unsigned LDS_BYTES = MSK_OFF + 1024;
static_assert(LDS_BYTES > 80 * 1024 && LDS_BYTES <= 160 * 1024, "exactly one workgroup per CU");
constexpr unsigned G_WS = 32768, WS_CTL = 16384, WS_DIG = WS_CTL + 512;
constexpr unsigned NOTREADY = 0xFFFFFFFFu;
constexpr unsigned BEAT_CAP = 1u << 18, POLL_CAP = 1u << 21;
constexpr float C2 = 0.088388347648318440f * 1.4426950408889634f;

__device__ __forceinline__ unsigned cvtpk(float lo, float hi) {
  bf16x2 v = {(__bf16)lo, (__bf16)hi};
  return __builtin_bit_cast(unsigned, v);
}
__device__ __forceinline__ float xg_max(float x) {
  auto s = __builtin_amdgcn_permlane16_swap(__float_as_uint(x), __float_as_uint(x), false, false);
  x = fmaxf(__uint_as_float(s[0]), __uint_as_float(s[1]));
  auto t = __builtin_amdgcn_permlane32_swap(__float_as_uint(x), __float_as_uint(x), false, false);
  return fmaxf(__uint_as_float(t[0]), __uint_as_float(t[1]));
}
__device__ __forceinline__ float xg_sum(float x) {
  auto s = __builtin_amdgcn_permlane16_swap(__float_as_uint(x), __float_as_uint(x), false, false);
  x = __uint_as_float(s[0]) + __uint_as_float(s[1]);
  auto t = __builtin_amdgcn_permlane32_swap(__float_as_uint(x), __float_as_uint(x), false, false);
  return __uint_as_float(t[0]) + __uint_as_float(t[1]);
}
__device__ __forceinline__ unsigned lsb4(u32x4 x) { return (x[0] & 1) | ((x[1] & 1) << 1) | ((x[2] & 1) << 2) | ((x[3] & 1) << 3); }

#define DMA_LOADS "buffer_load_dwordx4 %3, %1, %2 offen nt lds\n\tbuffer_load_dwordx4 %4, %1, %2 offen offset:1024 nt lds\n\t" \
                  "buffer_load_dwordx4 %5, %1, %2 offen offset:2048 nt lds\n\tbuffer_load_dwordx4 %6, %1, %2 offen offset:3072 nt lds"
#define DMA_ARGS(p, m0v, soff) "s"(m0v), "s"(__builtin_amdgcn_make_buffer_rsrc((void*)(p), 0, NPAIR * PAIR_BYTES, 0x00020000)), "s"(soff), \
                               "v"(so0), "v"(so1), "v"(so2), "v"(so3)
#define DMA_TILE(p, m0v, soff) asm volatile("s_mov_b32 m0, %0\n\ts_nop 0\n\t" DMA_LOADS :: DMA_ARGS(p, m0v, soff) : "memory")
#define WAIT_VM(n) asm volatile("s_waitcnt vmcnt(" #n ")" ::: "memory")
#define LOAD3(x1, x2, x3) "global_load_dwordx4 " x1 " sc1\n\tglobal_load_dwordx4 " x2 " sc1\n\tglobal_load_dwordx4 " x3 " sc1\n\t"

__global__ void __launch_bounds__(320, 2) gqa_decode(const char* __restrict__ K, const char* __restrict__ V, const char* __restrict__ Q, char* ws, unsigned* __restrict__ out) {
  extern __shared__ __attribute__((aligned(256))) char lds[];
  const unsigned bid = blockIdx.x, pair = (bid >> 3) * 2 + (((bid >> 1) ^ (bid & (bid >> 3))) & 1), j = (bid & 1) | ((bid >> 1) & 2);
  const unsigned lane = threadIdx.x & 63, w = __builtin_amdgcn_readfirstlane(threadIdx.x >> 6);
  const unsigned g = lane >> 4, c = lane & 15;
  unsigned so0 = g * 256 + ((c ^ (g << 2)) << 4), so1 = (so0 ^ 32) + 3072, so2 = (so0 ^ 48) + 2 * 3072, so3 = (so0 ^ 16) + 3 * 3072;
  unsigned qo = lane * 16;
  const unsigned ldsb = (unsigned)(size_t)lds;
  const unsigned ring = ldsb + w * RING_BYTES;
  const unsigned woff = pair * PAIR_BYTES + w * 1024, soff0 = woff + j * UNIT_BYTES;
  asm volatile("" : "+v"(so0), "+v"(so1), "+v"(so2), "+v"(so3), "+v"(qo) : "s"(ring), "s"(soff0));
  __builtin_amdgcn_sched_barrier(0);
  asm volatile("" :: "s"(K), "s"(V), "s"(Q), "s"(ws), "s"(out));
  asm volatile("s_mov_b32 m0, %0\n\ts_cmp_eq_u32 %7, %8\n\ts_cbranch_scc1 1f\n\t" DMA_LOADS "\n1:"
               :: DMA_ARGS(K, ring, soff0), "s"(w), "i"(NW) : "memory", "scc");
  __builtin_amdgcn_sched_barrier(0);
  char* const gws = ws + (size_t)pair * G_WS;

  if (w == NW) {
    volatile __attribute__((address_space(3))) unsigned* const tk = LDSP(volatile unsigned, ldsb + TK_OFF);
    __builtin_amdgcn_s_sleep(64);
    tk[lane] = lane == 63 ? 0u : NOTREADY;
    __builtin_amdgcn_s_setprio(3);
    gu32* const ctl = (gu32*)(gws + WS_CTL);
    const unsigned ma = ldsb + MSK_OFF;
    unsigned e0; u32x4 a0, a1, a2, a3;
    asm volatile("global_load_dword %0, %5, %7 sc1\n\tglobal_load_dwordx4 %1, %6, %7 sc1\n\t"
                 LOAD3("%2, %6, %7 offset:1024", "%3, %6, %7 offset:2048", "%4, %6, %7 offset:3072") "s_waitcnt vmcnt(0)"
                 : "=&v"(e0), "=&v"(a0), "=&v"(a1), "=&v"(a2), "=&v"(a3) : "v"(WS_CTL + (lane & 7) * 4), "v"(lane * 16 + j * 4096), "s"(gws) : "memory");
    const unsigned c0 = __builtin_amdgcn_readlane(e0, 4);
    const unsigned d0 = lsb4(a0) | (lsb4(a1) << 4) | (lsb4(a2) << 8) | (lsb4(a3) << 12);
    *LDSP(volatile unsigned short, ma + lane * 2) = (unsigned short)d0;
    const u32x4 dg = *LDSP(volatile u32x4, ma + (lane & 7) * 16);
    __builtin_amdgcn_raw_buffer_store_b128(dg, __builtin_amdgcn_make_buffer_rsrc((void*)(gws + WS_DIG + j * 128), 0, 128, 0x00020000), lane * 16, 0, 16);
    WAIT_VM(0);
    if (lane == 0) __hip_atomic_fetch_add(ctl + j, 1u, RLX_AGENT);
    bool fail = true;
#pragma unroll 1
    for (unsigned n = 0; n < BEAT_CAP; ++n) {
      __builtin_amdgcn_s_sleep(64);
      const unsigned e = __hip_atomic_load(ctl + (lane & 7), RLX_AGENT);
      if (__builtin_amdgcn_ballot_w64(lane < 4 && lane != j && e == e0) == 0) { fail = false; break; }
    }
    if (lane == 0) __hip_atomic_fetch_add(ctl + j, 1u, RLX_AGENT);
    unsigned d1, d2, d3;
    asm volatile("global_load_dword %0, %3, %6 sc1\n\tglobal_load_dword %1, %4, %6 sc1\n\tglobal_load_dword %2, %5, %6 sc1\n\ts_waitcnt vmcnt(0)"
                 : "=&v"(d1), "=&v"(d2), "=&v"(d3)
                 : "v"(WS_DIG + (j ^ 1) * 128 + (lane >> 1) * 4), "v"(WS_DIG + (j ^ 2) * 128 + (lane >> 1) * 4), "v"(WS_DIG + (j ^ 3) * 128 + (lane >> 1) * 4), "s"(gws) : "memory");
    d1 >>= (lane & 1) * 16; d2 >>= (lane & 1) * 16; d3 >>= (lane & 1) * 16;
#pragma unroll
    for (int y = 0; y < NW; ++y)
      *LDSP(volatile unsigned, ma + y * 256 + lane * 4) = ((d1 >> 4 * y) & 15) | (((d2 >> 4 * y) & 15) << 4) | (((d3 >> 4 * y) & 15) << 8) | (((d0 >> 4 * y) & 15) << 12);
    if (fail) out[(pair * 4 + j) * 64 + lane] = ~0u;
    asm volatile("s_waitcnt lgkmcnt(0)\n\ts_barrier" ::: "memory");
#pragma unroll 1
    for (unsigned k = 0;; ++k) {
      while (__builtin_amdgcn_readfirstlane(tk[63]) < 2 * (S_IT - 1) - LEAD + 2 * k) __builtin_amdgcn_s_sleep(1);
      unsigned t = NDYN;
      if (!fail) {
        unsigned tv = 0;
        if (lane == 0) tv = __hip_atomic_fetch_add(ctl + 4, 1u, RLX_AGENT);
        t = min(__builtin_amdgcn_readfirstlane(tv) - c0, NDYN);
        if (t == NDYN - 1) tk[k++] = t++;
      }
      tk[k] = t;
      if (t == NDYN) break;
    }
    asm volatile("s_waitcnt lgkmcnt(0)\n\ts_barrier" ::: "memory");
    return;
  }

  const unsigned qst = ldsb + SC_OFF + (w + 1) * SC_BYTES - 1024;
  asm volatile("s_mov_b32 m0, %0\n\ts_nop 0\n\tglobal_load_lds_dwordx4 %1, %2" :: "s"(qst), "v"(qo), "s"(Q + (size_t)pair * 1024) : "memory");
  DMA_TILE(K, ring + TILE_BYTES, soff0 + ROUND_BYTES);
  DMA_TILE(K, ring + 2 * TILE_BYTES, soff0 + 2 * ROUND_BYTES);

  const unsigned fc = ((c & 3) << 2) | ((((c >> 2) ^ (c >> 3)) & 1) << 1) | (c >> 3);
  const unsigned ka = c * 256 + ((g ^ fc) << 4);
  const unsigned qq = (lane >> 2) & 3, pp = lane & 3;
  const unsigned fr = (qq << 2) | (((g ^ (g >> 1)) & 1) << 1) | (g >> 1);
  const unsigned va = (4 * g + qq) * 256 + (((pp >> 1) ^ fr) << 4) + 8 * (pp & 1);
  const unsigned sa = ldsb + SC_OFF + w * SC_BYTES + (c & 3) * 64 + g * 16;
  float m = -INFINITY, mrun = -INFINITY, l = 0.f;
  unsigned rd = ring, wr = ring;
  u32x4 q0, q1, q2, q3;
  asm volatile("s_waitcnt vmcnt(8)\n\tds_read_b128 %0, %4\n\tds_read_b128 %1, %4 offset:64\n\tds_read_b128 %2, %4 offset:128\n\t"
               "ds_read_b128 %3, %4 offset:192\n\ts_waitcnt lgkmcnt(0)"
               : "=&v"(q0), "=&v"(q1), "=&v"(q2), "=&v"(q3) : "v"(qst + (c & 3) * 256 + g * 16) : "memory");

#define ADVANCE() do { wr = rd; rd += TILE_BYTES; if (rd == ring + RING_BYTES) rd = ring; } while (0)
#define PROGRESS(h) asm volatile("s_mov_b64 exec, 1\n\tds_max_u32 %0, %1\n\ts_mov_b64 exec, -1" :: "v"(ldsb + TK_OFF + 252), "v"(h) : "memory")
#define K_ADDR() unsigned a0 = rd + ka, a1 = a0 ^ 64, a2 = a0 ^ 128, a3 = a0 ^ 192; asm volatile("" : "+v"(a0), "+v"(a1), "+v"(a2), "+v"(a3))
#define K_STEP(sc) do {                    \
    u32x4 k0, k1, k2, k3;                                                                                           \
    asm volatile("ds_read_b128 %0, %4\n\tds_read_b128 %1, %5\n\tds_read_b128 %2, %6\n\tds_read_b128 %3, %7\n\ts_waitcnt lgkmcnt(0)" \
                 : "=&v"(k0), "=&v"(k1), "=&v"(k2), "=&v"(k3) : "v"(a0), "v"(a1), "v"(a2), "v"(a3) : "memory");      \
    f32x4 s = {0.f, 0.f, 0.f, 0.f};                                                                                 \
    s = __builtin_amdgcn_mfma_f32_16x16x32_bf16(__builtin_bit_cast(bf16x8, k0), __builtin_bit_cast(bf16x8, q0), s, 0, 0, 0); \
    s = __builtin_amdgcn_mfma_f32_16x16x32_bf16(__builtin_bit_cast(bf16x8, k1), __builtin_bit_cast(bf16x8, q1), s, 0, 0, 0); \
    s = __builtin_amdgcn_mfma_f32_16x16x32_bf16(__builtin_bit_cast(bf16x8, k2), __builtin_bit_cast(bf16x8, q2), s, 0, 0, 0); \
    s = __builtin_amdgcn_mfma_f32_16x16x32_bf16(__builtin_bit_cast(bf16x8, k3), __builtin_bit_cast(bf16x8, q3), s, 0, 0, 0); \
    mrun = fmaxf(mrun, fmaxf(fmaxf(s[0], s[1]), fmaxf(s[2], s[3])) * C2);                                           \
    *LDSP(f32x4, sc) = s;                                                                                           \
    ADVANCE();                                                                                                      \
  } while (0)

#pragma unroll 1
  for (int h = 0; h < S_SEP; ++h) {
    K_ADDR();
    if (h != 0) {
      const bool nv = h + 2 >= S_SEP;
      DMA_TILE(nv ? V : K, wr, soff0 + (unsigned)(nv ? h + 2 - S_SEP : h + 2) * ROUND_BYTES);
      WAIT_VM(8);
    }
    K_STEP(sa + 256 * h);
  }
  f32x4 acc0 = {0.f, 0.f, 0.f, 0.f}, acc1 = acc0;
  unsigned reca = ldsb + RED_OFF + (w * 4 + (c & 3)) * RED_REC + (c >> 2) * 64 + g * 16;
  unsigned recw = ldsb + RED_OFF + (w * 4 + (c & 3)) * RED_REC + DH * 4;
  unsigned recb = ldsb + RED_OFF + w * RED_REC + lane * 8;
  unsigned recm = ldsb + RED_OFF + w * RED_REC + DH * 4;
  unsigned mb0 = lane * 16 + j * 4096 + w * 1024, mb1 = mb0 ^ 4096, mb2 = mb0 ^ 8192, mb3 = mb0 ^ 12288;
  unsigned oo = ((pair * 4 + w) * 64 + lane) * 4;
  asm volatile("" : "+v"(reca), "+v"(recw), "+v"(recb), "+v"(recm), "+v"(mb0), "+v"(mb1), "+v"(mb2), "+v"(mb3), "+v"(oo));
  unsigned snap = 0;
  unsigned noff = 0;
  bool more = true, fin = false;
  u32x2 pk0, pk1, pk2, pk3; unsigned b0, b1, b2, b3, b4, b5, b6, b7;
#define PV(acc, va_, vb_, pa_, pb_) acc = __builtin_amdgcn_mfma_f32_16x16x32_bf16(__builtin_bit_cast(bf16x8, __builtin_shufflevector(va_, vb_, 0, 1, 2, 3)), \
                                                                            __builtin_bit_cast(bf16x8, __builtin_shufflevector(pa_, pb_, 0, 1, 2, 3)), acc, 0, 0, 0)
#define V_MMA() do {                                                                                                \
    u32x2 v0, v1, v2, v3, v4, v5, v6, v7;                                                                           \
    asm volatile("ds_read_b64_tr_b16 %0, %8\n\tds_read_b64_tr_b16 %1, %9\n\tds_read_b64_tr_b16 %2, %10\n\tds_read_b64_tr_b16 %3, %11\n\t" \
                 "ds_read_b64_tr_b16 %4, %12\n\tds_read_b64_tr_b16 %5, %13\n\tds_read_b64_tr_b16 %6, %14\n\tds_read_b64_tr_b16 %7, %15\n\t" \
                 "s_waitcnt lgkmcnt(0)"                                                                             \
                 : "=&v"(v0), "=&v"(v1), "=&v"(v2), "=&v"(v3), "=&v"(v4), "=&v"(v5), "=&v"(v6), "=&v"(v7)            \
                 : "v"(b0), "v"(b1), "v"(b2), "v"(b3), "v"(b4), "v"(b5), "v"(b6), "v"(b7) : "memory");               \
    PV(acc0, v0, v1, pk0, pk1); PV(acc1, v4, v5, pk0, pk1); PV(acc0, v2, v3, pk2, pk3); PV(acc1, v6, v7, pk2, pk3); \
  } while (0)
#pragma unroll 1
  for (int u = 0;; ++u) {
    if (u >= S_SEP) {
      K_ADDR();
      PROGRESS(2 * u);
      unsigned t;
      do t = __builtin_amdgcn_readfirstlane(*LDSP(volatile unsigned, ldsb + TK_OFF + 4 * (u - S_SEP))); while (t == NOTREADY);
      more = t < NDYN;
      fin |= t == NDYN - 1;
      noff = woff + (S_IT * GRP + t) * UNIT_BYTES;
      if (more) {
        DMA_TILE(K, wr, noff);
        WAIT_VM(8);
      } else {
        snap = *LDSP(volatile unsigned, ldsb + MSK_OFF + (w * 64 + lane) * 4);
        WAIT_VM(4);
      }
      K_STEP(sa);
    }
    if (u > U_SYNC) PROGRESS(u < S_SEP ? S_SEP + u : 2 * u + 1);
    b0 = rd + va; b1 = b0 ^ 32; b2 = b0 ^ 64; b3 = b0 ^ 96; b4 = b0 ^ 128; b5 = b0 ^ 160; b6 = b0 ^ 192; b7 = b0 ^ 224;
    if (__builtin_amdgcn_ballot_w64(mrun > m) != 0) {
      const float mn = xg_max(mrun);
      const float al = __builtin_amdgcn_exp2f(m - mn);
      m = mn; l *= al; acc0 *= al; acc1 *= al;
    }
    const f32x4 s = *LDSP(const f32x4, sa + 256 * (unsigned)(u < S_SEP ? u : 0));
    const float p0 = __builtin_amdgcn_exp2f(fmaf(s[0], C2, -m)), p1 = __builtin_amdgcn_exp2f(fmaf(s[1], C2, -m));
    const float p2 = __builtin_amdgcn_exp2f(fmaf(s[2], C2, -m)), p3 = __builtin_amdgcn_exp2f(fmaf(s[3], C2, -m));
    l += (p0 + p1) + (p2 + p3);
    const u32x2 pk = {cvtpk(p0, p1), cvtpk(p2, p3)}, pz = {0u, 0u};
    pk0 = (c >> 2) == 0 ? pk : pz; pk1 = (c >> 2) == 1 ? pk : pz; pk2 = (c >> 2) == 2 ? pk : pz; pk3 = (c >> 2) == 3 ? pk : pz;
    asm volatile("" : "+v"(pk0), "+v"(pk1), "+v"(pk2), "+v"(pk3), "+v"(b0), "+v"(b1), "+v"(b2), "+v"(b3), "+v"(b4), "+v"(b5), "+v"(b6), "+v"(b7));
    if (!more) break;
    DMA_TILE(u == S_SEP - 2 ? K : V, wr, u >= S_SEP ? noff : soff0 + (unsigned)min(u + 2, S_SEP) * ROUND_BYTES);
    if (u == U_SYNC) __builtin_amdgcn_s_barrier();
    WAIT_VM(8);
    V_MMA();
    ADVANCE();
  }

  *LDSP(f32x2, recw) = f32x2{m, xg_sum(l)};
  unsigned sb[12];
#pragma unroll
  for (int y = 0; y < 12; ++y) sb[y] = (fin ? snap : ~snap >> 12) >> y;
  asm volatile("" : "+v"(sb[0]), "+v"(sb[1]), "+v"(sb[2]), "+v"(sb[3]), "+v"(sb[4]), "+v"(sb[5]), "+v"(sb[6]), "+v"(sb[7]), "+v"(sb[8]), "+v"(sb[9]), "+v"(sb[10]), "+v"(sb[11]));
  u32x4 x[NPOLL][3];
#define POLL(k) asm volatile(LOAD3("%0, %3, %6", "%1, %4, %6", "%2, %5, %6")                                        \
                   : "=&v"(x[k][0]), "=&v"(x[k][1]), "=&v"(x[k][2]) : "v"(mb1), "v"(mb2), "v"(mb3), "s"(gws) : "memory")
  WAIT_VM(0);
  V_MMA();
  *LDSP(f32x4, reca) = acc0;
  *LDSP(f32x4, reca + 256) = acc1;
  if (fin) POLL(0);
  f32x2 ml[NW], o[NW];
  asm volatile("s_waitcnt lgkmcnt(0)\n\ts_barrier\n\t"
               "ds_read_b64 %0, %8\n\tds_read_b64 %1, %8 offset:%10\n\tds_read_b64 %2, %8 offset:%11\n\tds_read_b64 %3, %8 offset:%12\n\t"
               "ds_read_b64 %4, %9\n\tds_read_b64 %5, %9 offset:%10\n\tds_read_b64 %6, %9 offset:%11\n\tds_read_b64 %7, %9 offset:%12\n\t"
               "s_waitcnt lgkmcnt(0)"
               : "=&v"(ml[0]), "=&v"(ml[1]), "=&v"(ml[2]), "=&v"(ml[3]), "=&v"(o[0]), "=&v"(o[1]), "=&v"(o[2]), "=&v"(o[3])
               : "v"(recm), "v"(recb), "i"(4 * RED_REC), "i"(8 * RED_REC), "i"(12 * RED_REC) : "memory");
  u32x4 r;
  {
    const float M = __uint_as_float(__float_as_uint(fmaxf(fmaxf(ml[0][0], ml[1][0]), fmaxf(ml[2][0], ml[3][0]))) & ~1u);
    const float a0 = __builtin_amdgcn_exp2f(ml[0][0] - M), a1 = __builtin_amdgcn_exp2f(ml[1][0] - M), a2 = __builtin_amdgcn_exp2f(ml[2][0] - M), a3 = __builtin_amdgcn_exp2f(ml[3][0] - M);
    const float o0 = fmaf(a0, o[0][0], a1 * o[1][0]) + fmaf(a2, o[2][0], a3 * o[3][0]), o1 = fmaf(a0, o[0][1], a1 * o[1][1]) + fmaf(a2, o[2][1], a3 * o[3][1]);
    const float L = fmaf(a0, ml[0][1], a1 * ml[1][1]) + fmaf(a2, ml[2][1], a3 * ml[3][1]);
    r = u32x4{__float_as_uint(o0) & ~1u, __float_as_uint(o1) & ~1u, __float_as_uint(M), __float_as_uint(L) & ~1u};
  }
  if (!fin) {
    const u32x4 u = {r[0] | (sb[0] & 1), r[1] | (sb[1] & 1), r[2] | (sb[2] & 1), r[3] | (sb[3] & 1)};
    __builtin_amdgcn_raw_buffer_store_b128(u, __builtin_amdgcn_make_buffer_rsrc((void*)gws, 0, WS_CTL, 0x00020000), mb0, 0, 16);
  } else {
    POLL(1);
    u32x4 x1, x2, x3;
    asm("" : "=v"(x1), "=v"(x2), "=v"(x3));
    bool done = false;
#pragma unroll 1
    for (unsigned n = 0; n < POLL_CAP && !done; ++n) {
#pragma unroll
      for (int k = 0; k < NPOLL; ++k) {
        asm volatile("s_waitcnt vmcnt(%3)" : "+v"(x[k][0]), "+v"(x[k][1]), "+v"(x[k][2]) : "i"(3 * (NPOLL - 1)) : "memory");
        unsigned fresh = 1;
#pragma unroll
        for (int y = 0; y < 12; ++y) fresh &= x[k][y >> 2][y & 3] ^ sb[y];
        if (__builtin_amdgcn_ballot_w64(fresh == 0) == 0) { x1 = x[k][0]; x2 = x[k][1]; x3 = x[k][2]; done = true; break; }
        POLL(k);
      }
    }
    if (!done) r[3] = ~0u;
    const f32x4 r0 = __builtin_bit_cast(f32x4, r), r1 = __builtin_bit_cast(f32x4, x1 & ~1u), r2 = __builtin_bit_cast(f32x4, x2 & ~1u), r3 = __builtin_bit_cast(f32x4, x3 & ~1u);
    const float M = fmaxf(fmaxf(r0[2], r1[2]), fmaxf(r2[2], r3[2]));
    const float a0 = __builtin_amdgcn_exp2f(r0[2] - M), a1 = __builtin_amdgcn_exp2f(r1[2] - M), a2 = __builtin_amdgcn_exp2f(r2[2] - M), a3 = __builtin_amdgcn_exp2f(r3[2] - M);
    const float L = fmaf(a0, r0[3], a1 * r1[3]) + fmaf(a2, r2[3], a3 * r3[3]);
    const float y0 = fmaf(a0, r0[0], a1 * r1[0]) + fmaf(a2, r2[0], a3 * r3[0]);
    const float y1 = fmaf(a0, r0[1], a1 * r1[1]) + fmaf(a2, r2[1], a3 * r3[1]);
    const float inv = __builtin_amdgcn_rcpf(L);
    *(unsigned*)((char*)out + oo) = cvtpk(y0 * inv, y1 * inv);
#pragma unroll
    for (int k = 0; k < NPOLL; ++k) asm volatile("s_waitcnt vmcnt(0)" : "+v"(x[k][0]), "+v"(x[k][1]), "+v"(x[k][2]) :: "memory");
  }
}

extern "C" void kernel_launch(void* const* d_in, const int* in_sizes, int n_in, void* d_out, int out_size, void* d_ws, size_t ws_size, hipStream_t stream) {
  (void)hipFuncSetAttribute((const void*)gqa_decode, hipFuncAttributeMaxDynamicSharedMemorySize, LDS_BYTES);
  gqa_decode<<<NPAIR * GRP, (NW + 1) * 64, LDS_BYTES, stream>>>((const char*)d_in[1], (const char*)d_in[2], (const char*)d_in[0], (char*)d_ws, (unsigned*)d_out);
}
```

```cpp
#include <hip/hip_runtime.h>
#include <hip/hip_bf16.h>

typedef unsigned u32x4 __attribute__((ext_vector_type(4)));
typedef unsigned u32x2 __attribute__((ext_vector_type(2)));
typedef float f32x4 __attribute__((ext_vector_type(4)));
typedef float f32x2 __attribute__((ext_vector_type(2)));
typedef __bf16 bf16x8 __attribute__((ext_vector_type(8)));
typedef __bf16 bf16x2 __attribute__((ext_vector_type(2)));
#define LDSP(T, a) ((__attribute__((address_space(3))) T*)(size_t)(a))
typedef __attribute__((address_space(1))) unsigned gu32;
#define RLX_AGENT __ATOMIC_RELAXED, __HIP_MEMORY_SCOPE_AGENT

constexpr int SK = 8192, DH = 128, NPAIR = 64, GRP = 4, NW = 4;
constexpr int TILE = 16;
constexpr int NUNIT = SK / (TILE * NW);
constexpr int S_IT = 27;
constexpr int S_SEP = S_IT - 1;
constexpr unsigned NDYN = NUNIT - S_IT * GRP;
constexpr int LEAD = 2;
constexpr int U_SYNC = S_SEP - 8;
constexpr int NPOLL = 2;
static_assert(S_SEP >= 3 && S_SEP <= 28 && NDYN < 60, "score store: 28 tiles + q per wave; ticket table: one LDS word per lane");
constexpr unsigned PAIR_BYTES = SK * DH * 2;
constexpr unsigned TILE_BYTES = TILE * DH * 2;
constexpr unsigned UNIT_BYTES = NW * TILE_BYTES;
constexpr unsigned ROUND_BYTES = GRP * UNIT_BYTES;
constexpr unsigned RING_BYTES = 3 * TILE_BYTES;
constexpr unsigned SC_OFF = NW * RING_BYTES, SC_BYTES = 32 * 256;
constexpr unsigned RED_OFF = SC_OFF + NW * SC_BYTES;
constexpr unsigned RED_REC = (DH + 4) * 4;
constexpr unsigned TK_OFF = RED_OFF + NW * 4 * RED_REC;
constexpr unsigned MSK_OFF = TK_OFF + 256;
constexpr unsigned LDS_BYTES = MSK_OFF + 1024;
static_assert(LDS_BYTES > 80 * 1024 && LDS_BYTES <= 160 * 1024, "exactly one workgroup per CU");
constexpr unsigned G_WS = 32768, WS_CTL = 16384, WS_DIG = WS_CTL + 512;
constexpr unsigned NOTREADY = 0xFFFFFFFFu;
constexpr unsigned BEAT_CAP = 1u << 18, POLL_CAP = 1u << 21;
constexpr float C2 = 0.088388347648318440f * 1.4426950408889634f;

__device__ __forceinline__ unsigned cvtpk(float lo, float hi) {
  bf16x2 v = {(__bf16)lo, (__bf16)hi};
  return __builtin_bit_cast(unsigned, v);
}
__device__ __forceinline__ float xg_max(float x) {
  auto s = __builtin_amdgcn_permlane16_swap(__float_as_uint(x), __float_as_uint(x), false, false);
  x = fmaxf(__uint_as_float(s[0]), __uint_as_float(s[1]));
  auto t = __builtin_amdgcn_permlane32_swap(__float_as_uint(x), __float_as_uint(x), false, false);
  return fmaxf(__uint_as_float(t[0]), __uint_as_float(t[1]));
}
__device__ __forceinline__ float xg_sum(float x) {
  auto s = __builtin_amdgcn_permlane16_swap(__float_as_uint(x), __float_as_uint(x), false, false);
  x = __uint_as_float(s[0]) + __uint_as_float(s[1]);
  auto t = __builtin_amdgcn_permlane32_swap(__float_as_uint(x), __float_as_uint(x), false, false);
  return __uint_as_float(t[0]) + __uint_as_float(t[1]);
}
__device__ __forceinline__ unsigned lsb4(u32x4 x) { return (x[0] & 1) | ((x[1] & 1) << 1) | ((x[2] & 1) << 2) | ((x[3] & 1) << 3); }

#define DMA_LOADS "buffer_load_dwordx4 %3, %1, %2 offen nt lds\n\tbuffer_load_dwordx4 %4, %1, %2 offen offset:1024 nt lds\n\t" \
                  "buffer_load_dwordx4 %5, %1, %2 offen offset:2048 nt lds\n\tbuffer_load_dwordx4 %6, %1, %2 offen offset:3072 nt lds"
#define DMA_ARGS(p, m0v, soff) "s"(m0v), "s"(__builtin_amdgcn_make_buffer_rsrc((void*)(p), 0, NPAIR * PAIR_BYTES, 0x00020000)), "s"(soff), \
                               "v"(so0), "v"(so1), "v"(so2), "v"(so3)
#define DMA_TILE(p, m0v, soff) asm volatile("s_mov_b32 m0, %0\n\ts_nop 0\n\t" DMA_LOADS :: DMA_ARGS(p, m0v, soff) : "memory")
#define WAIT_VM(n) asm volatile("s_waitcnt vmcnt(" #n ")" ::: "memory")
#define LOAD3(x1, x2, x3) "global_load_dwordx4 " x1 " sc1\n\tglobal_load_dwordx4 " x2 " sc1\n\tglobal_load_dwordx4 " x3 " sc1\n\t"

__global__ void __launch_bounds__(320, 2) gqa_decode(const char* __restrict__ K, const char* __restrict__ V, const char* __restrict__ Q, char* ws, unsigned* __restrict__ out) {
  extern __shared__ __attribute__((aligned(256))) char lds[];
  unsigned pfd;
  {
    unsigned long long pc;
    asm volatile("s_getpc_b64 %0" : "=s"(pc));
    asm volatile("global_load_dword %0, %1, %2" : "=v"(pfd) : "v"(min(threadIdx.x * 16, 32u)), "s"(pc) : "memory");
  }
  const unsigned bid = blockIdx.x, pair = (bid >> 3) * 2 + (((bid >> 1) ^ (bid & (bid >> 3))) & 1), j = (bid & 1) | ((bid >> 1) & 2);
  const unsigned lane = threadIdx.x & 63, w = __builtin_amdgcn_readfirstlane(threadIdx.x >> 6);
  const unsigned g = lane >> 4, c = lane & 15;
  unsigned so0 = g * 256 + ((c ^ (g << 2)) << 4), so1 = (so0 ^ 32) + 3072, so2 = (so0 ^ 48) + 2 * 3072, so3 = (so0 ^ 16) + 3 * 3072;
  unsigned qo = lane * 16;
  const unsigned ldsb = (unsigned)(size_t)lds;
  const unsigned ring = ldsb + w * RING_BYTES;
  const unsigned woff = pair * PAIR_BYTES + w * 1024, soff0 = woff + j * UNIT_BYTES;
  asm volatile("" : "+v"(so0), "+v"(so1), "+v"(so2), "+v"(so3), "+v"(qo) : "s"(ring), "s"(soff0));
  __builtin_amdgcn_sched_barrier(0);
  asm volatile("" :: "s"(K), "s"(V), "s"(Q), "s"(ws), "s"(out));
  asm volatile("s_mov_b32 m0, %0\n\ts_cmp_eq_u32 %7, %8\n\ts_cbranch_scc1 1f\n\t" DMA_LOADS "\n1:"
               :: DMA_ARGS(K, ring, soff0), "s"(w), "i"(NW) : "memory", "scc");
  __builtin_amdgcn_sched_barrier(0);
  char* const gws = ws + (size_t)pair * G_WS;

  if (w == NW) {
    volatile __attribute__((address_space(3))) unsigned* const tk = LDSP(volatile unsigned, ldsb + TK_OFF);
    __builtin_amdgcn_s_sleep(64);
    tk[lane] = lane == 63 ? 0u : NOTREADY;
    __builtin_amdgcn_s_setprio(3);
    asm volatile("s_waitcnt vmcnt(0)" : "+v"(pfd) :: "memory");
    gu32* const ctl = (gu32*)(gws + WS_CTL);
    const unsigned ma = ldsb + MSK_OFF;
    unsigned e0; u32x4 a0, a1, a2, a3;
    asm volatile("global_load_dword %0, %5, %7 sc1\n\tglobal_load_dwordx4 %1, %6, %7 sc1\n\t"
                 LOAD3("%2, %6, %7 offset:1024", "%3, %6, %7 offset:2048", "%4, %6, %7 offset:3072") "s_waitcnt vmcnt(0)"
                 : "=&v"(e0), "=&v"(a0), "=&v"(a1), "=&v"(a2), "=&v"(a3) : "v"(WS_CTL + (lane & 7) * 4), "v"(lane * 16 + j * 4096), "s"(gws) : "memory");
    const unsigned c0 = __builtin_amdgcn_readlane(e0, 4);
    const unsigned d0 = lsb4(a0) | (lsb4(a1) << 4) | (lsb4(a2) << 8) | (lsb4(a3) << 12);
    *LDSP(volatile unsigned short, ma + lane * 2) = (unsigned short)d0;
    const u32x4 dg = *LDSP(volatile u32x4, ma + (lane & 7) * 16);
    __builtin_amdgcn_raw_buffer_store_b128(dg, __builtin_amdgcn_make_buffer_rsrc((void*)(gws + WS_DIG + j * 128), 0, 128, 0x00020000), lane * 16, 0, 16);
    WAIT_VM(0);
    if (lane == 0) __hip_atomic_fetch_add(ctl + j, 1u, RLX_AGENT);
    bool fail = true;
#pragma unroll 1
    for (unsigned n = 0; n < BEAT_CAP; ++n) {
      __builtin_amdgcn_s_sleep(64);
      const unsigned e = __hip_atomic_load(ctl + (lane & 7), RLX_AGENT);
      if (__builtin_amdgcn_ballot_w64(lane < 4 && lane != j && e == e0) == 0) { fail = false; break; }
    }
    if (lane == 0) __hip_atomic_fetch_add(ctl + j, 1u, RLX_AGENT);
    unsigned d1, d2, d3;
    asm volatile("global_load_dword %0, %3, %6 sc1\n\tglobal_load_dword %1, %4, %6 sc1\n\tglobal_load_dword %2, %5, %6 sc1\n\ts_waitcnt vmcnt(0)"
                 : "=&v"(d1), "=&v"(d2), "=&v"(d3)
                 : "v"(WS_DIG + (j ^ 1) * 128 + (lane >> 1) * 4), "v"(WS_DIG + (j ^ 2) * 128 + (lane >> 1) * 4), "v"(WS_DIG + (j ^ 3) * 128 + (lane >> 1) * 4), "s"(gws) : "memory");
    d1 >>= (lane & 1) * 16; d2 >>= (lane & 1) * 16; d3 >>= (lane & 1) * 16;
#pragma unroll
    for (int y = 0; y < NW; ++y)
      *LDSP(volatile unsigned, ma + y * 256 + lane * 4) = ((d1 >> 4 * y) & 15) | (((d2 >> 4 * y) & 15) << 4) | (((d3 >> 4 * y) & 15) << 8) | (((d0 >> 4 * y) & 15) << 12);
    if (fail) out[(pair * 4 + j) * 64 + lane] = ~0u;
    asm volatile("s_waitcnt lgkmcnt(0)\n\ts_barrier" ::: "memory");
#pragma unroll 1
    for (unsigned k = 0;; ++k) {
      while (__builtin_amdgcn_readfirstlane(tk[63]) < 2 * (S_IT - 1) - LEAD + 2 * k) __builtin_amdgcn_s_sleep(1);
      unsigned t = NDYN;
      if (!fail) {
        unsigned tv = 0;
        if (lane == 0) tv = __hip_atomic_fetch_add(ctl + 4, 1u, RLX_AGENT);
        t = min(__builtin_amdgcn_readfirstlane(tv) - c0, NDYN);
        if (t == NDYN - 1) tk[k++] = t++;
      }
      tk[k] = t;
      if (t == NDYN) break;
    }
    asm volatile("s_waitcnt lgkmcnt(0)\n\ts_barrier" ::: "memory");
    return;
  }

  const unsigned qst = ldsb + SC_OFF + (w + 1) * SC_BYTES - 1024;
  asm volatile("s_mov_b32 m0, %0\n\ts_nop 0\n\tglobal_load_lds_dwordx4 %1, %2" :: "s"(qst), "v"(qo), "s"(Q + (size_t)pair * 1024) : "memory");
  DMA_TILE(K, ring + TILE_BYTES, soff0 + ROUND_BYTES);
  DMA_TILE(K, ring + 2 * TILE_BYTES, soff0 + 2 * ROUND_BYTES);

  const unsigned fc = ((c & 3) << 2) | ((((c >> 2) ^ (c >> 3)) & 1) << 1) | (c >> 3);
  const unsigned ka = c * 256 + ((g ^ fc) << 4);
  const unsigned qq = (lane >> 2) & 3, pp = lane & 3;
  const unsigned fr = (qq << 2) | (((g ^ (g >> 1)) & 1) << 1) | (g >> 1);
  const unsigned va = (4 * g + qq) * 256 + (((pp >> 1) ^ fr) << 4) + 8 * (pp & 1);
  const unsigned sa = ldsb + SC_OFF + w * SC_BYTES + (c & 3) * 64 + g * 16;
  float m = -INFINITY, mrun = -INFINITY, l = 0.f;
  unsigned rd = ring, wr = ring;
  u32x4 q0, q1, q2, q3;
  asm volatile("s_waitcnt vmcnt(8)\n\tds_read_b128 %0, %4\n\tds_read_b128 %1, %4 offset:64\n\tds_read_b128 %2, %4 offset:128\n\t"
               "ds_read_b128 %3, %4 offset:192\n\ts_waitcnt lgkmcnt(0)"
               : "=&v"(q0), "=&v"(q1), "=&v"(q2), "=&v"(q3) : "v"(qst + (c & 3) * 256 + g * 16) : "memory");

  asm volatile("" : "+v"(pfd));
#define ADVANCE() do { wr = rd; rd += TILE_BYTES; if (rd == ring + RING_BYTES) rd = ring; } while (0)
#define PROGRESS(h) asm volatile("s_mov_b64 exec, 1\n\tds_max_u32 %0, %1\n\ts_mov_b64 exec, -1" :: "v"(ldsb + TK_OFF + 252), "v"(h) : "memory")
#define K_ADDR() unsigned a0 = rd + ka, a1 = a0 ^ 64, a2 = a0 ^ 128, a3 = a0 ^ 192; asm volatile("" : "+v"(a0), "+v"(a1), "+v"(a2), "+v"(a3))
#define K_STEP(sc) do {                    \
    u32x4 k0, k1, k2, k3;                                                                                           \
    asm volatile("ds_read_b128 %0, %4\n\tds_read_b128 %1, %5\n\tds_read_b128 %2, %6\n\tds_read_b128 %3, %7\n\ts_waitcnt lgkmcnt(0)" \
                 : "=&v"(k0), "=&v"(k1), "=&v"(k2), "=&v"(k3) : "v"(a0), "v"(a1), "v"(a2), "v"(a3) : "memory");      \
    f32x4 s = {0.f, 0.f, 0.f, 0.f};                                                                                 \
    s = __builtin_amdgcn_mfma_f32_16x16x32_bf16(__builtin_bit_cast(bf16x8, k0), __builtin_bit_cast(bf16x8, q0), s, 0, 0, 0); \
    s = __builtin_amdgcn_mfma_f32_16x16x32_bf16(__builtin_bit_cast(bf16x8, k1), __builtin_bit_cast(bf16x8, q1), s, 0, 0, 0); \
    s = __builtin_amdgcn_mfma_f32_16x16x32_bf16(__builtin_bit_cast(bf16x8, k2), __builtin_bit_cast(bf16x8, q2), s, 0, 0, 0); \
    s = __builtin_amdgcn_mfma_f32_16x16x32_bf16(__builtin_bit_cast(bf16x8, k3), __builtin_bit_cast(bf16x8, q3), s, 0, 0, 0); \
    mrun = fmaxf(mrun, fmaxf(fmaxf(s[0], s[1]), fmaxf(s[2], s[3])) * C2);                                           \
    *LDSP(f32x4, sc) = s;                                                                                           \
    ADVANCE();                                                                                                      \
  } while (0)

#pragma unroll 1
  for (int h = 0; h < S_SEP; ++h) {
    K_ADDR();
    if (h != 0) {
      const bool nv = h + 2 >= S_SEP;
      DMA_TILE(nv ? V : K, wr, soff0 + (unsigned)(nv ? h + 2 - S_SEP : h + 2) * ROUND_BYTES);
      WAIT_VM(8);
    }
    K_STEP(sa + 256 * h);
  }
  f32x4 acc0 = {0.f, 0.f, 0.f, 0.f}, acc1 = acc0;
  unsigned reca = ldsb + RED_OFF + (w * 4 + (c & 3)) * RED_REC + (c >> 2) * 64 + g * 16;
  unsigned recw = ldsb + RED_OFF + (w * 4 + (c & 3)) * RED_REC + DH * 4;
  unsigned recb = ldsb + RED_OFF + w * RED_REC + lane * 8;
  unsigned recm = ldsb + RED_OFF + w * RED_REC + DH * 4;
  unsigned mb0 = lane * 16 + j * 4096 + w * 1024, mb1 = mb0 ^ 4096, mb2 = mb0 ^ 8192, mb3 = mb0 ^ 12288;
  unsigned oo = ((pair * 4 + w) * 64 + lane) * 4;
  asm volatile("" : "+v"(reca), "+v"(recw), "+v"(recb), "+v"(recm), "+v"(mb0), "+v"(mb1), "+v"(mb2), "+v"(mb3), "+v"(oo));
  unsigned snap = 0;
  unsigned noff = 0;
  bool more = true, fin = false;
  u32x2 pk0, pk1, pk2, pk3; unsigned b0, b1, b2, b3, b4, b5, b6, b7;
#define PV(acc, va_, vb_, pa_, pb_) acc = __builtin_amdgcn_mfma_f32_16x16x32_bf16(__builtin_bit_cast(bf16x8, __builtin_shufflevector(va_, vb_, 0, 1, 2, 3)), \
                                                                            __builtin_bit_cast(bf16x8, __builtin_shufflevector(pa_, pb_, 0, 1, 2, 3)), acc, 0, 0, 0)
#define V_MMA() do {                                                                                                \
    u32x2 v0, v1, v2, v3, v4, v5, v6, v7;                                                                           \
    asm volatile("ds_read_b64_tr_b16 %0, %8\n\tds_read_b64_tr_b16 %1, %9\n\tds_read_b64_tr_b16 %2, %10\n\tds_read_b64_tr_b16 %3, %11\n\t" \
                 "ds_read_b64_tr_b16 %4, %12\n\tds_read_b64_tr_b16 %5, %13\n\tds_read_b64_tr_b16 %6, %14\n\tds_read_b64_tr_b16 %7, %15\n\t" \
                 "s_waitcnt lgkmcnt(0)"                                                                             \
                 : "=&v"(v0), "=&v"(v1), "=&v"(v2), "=&v"(v3), "=&v"(v4), "=&v"(v5), "=&v"(v6), "=&v"(v7)            \
                 : "v"(b0), "v"(b1), "v"(b2), "v"(b3), "v"(b4), "v"(b5), "v"(b6), "v"(b7) : "memory");               \
    PV(acc0, v0, v1, pk0, pk1); PV(acc1, v4, v5, pk0, pk1); PV(acc0, v2, v3, pk2, pk3); PV(acc1, v6, v7, pk2, pk3); \
  } while (0)
#pragma unroll 1
  for (int u = 0;; ++u) {
    if (u >= S_SEP) {
      K_ADDR();
      PROGRESS(2 * u);
      unsigned t;
      do t = __builtin_amdgcn_readfirstlane(*LDSP(volatile unsigned, ldsb + TK_OFF + 4 * (u - S_SEP))); while (t == NOTREADY);
      more = t < NDYN;
      fin |= t == NDYN - 1;
      noff = woff + (S_IT * GRP + t) * UNIT_BYTES;
      if (more) {
        DMA_TILE(K, wr, noff);
        WAIT_VM(8);
      } else {
        snap = *LDSP(volatile unsigned, ldsb + MSK_OFF + (w * 64 + lane) * 4);
        WAIT_VM(4);
      }
      K_STEP(sa);
    }
    if (u > U_SYNC) PROGRESS(u < S_SEP ? S_SEP + u : 2 * u + 1);
    b0 = rd + va; b1 = b0 ^ 32; b2 = b0 ^ 64; b3 = b0 ^ 96; b4 = b0 ^ 128; b5 = b0 ^ 160; b6 = b0 ^ 192; b7 = b0 ^ 224;
    if (__builtin_amdgcn_ballot_w64(mrun > m) != 0) {
      const float mn = xg_max(mrun);
      const float al = __builtin_amdgcn_exp2f(m - mn);
      m = mn; l *= al; acc0 *= al; acc1 *= al;
    }
    const f32x4 s = *LDSP(const f32x4, sa + 256 * (unsigned)(u < S_SEP ? u : 0));
    const float p0 = __builtin_amdgcn_exp2f(fmaf(s[0], C2, -m)), p1 = __builtin_amdgcn_exp2f(fmaf(s[1], C2, -m));
    const float p2 = __builtin_amdgcn_exp2f(fmaf(s[2], C2, -m)), p3 = __builtin_amdgcn_exp2f(fmaf(s[3], C2, -m));
    l += (p0 + p1) + (p2 + p3);
    const u32x2 pk = {cvtpk(p0, p1), cvtpk(p2, p3)}, pz = {0u, 0u};
    pk0 = (c >> 2) == 0 ? pk : pz; pk1 = (c >> 2) == 1 ? pk : pz; pk2 = (c >> 2) == 2 ? pk : pz; pk3 = (c >> 2) == 3 ? pk : pz;
    asm volatile("" : "+v"(pk0), "+v"(pk1), "+v"(pk2), "+v"(pk3), "+v"(b0), "+v"(b1), "+v"(b2), "+v"(b3), "+v"(b4), "+v"(b5), "+v"(b6), "+v"(b7));
    if (!more) break;
    DMA_TILE(u == S_SEP - 2 ? K : V, wr, u >= S_SEP ? noff : soff0 + (unsigned)min(u + 2, S_SEP) * ROUND_BYTES);
    if (u == U_SYNC) __builtin_amdgcn_s_barrier();
    WAIT_VM(8);
    V_MMA();
    ADVANCE();
  }

  *LDSP(f32x2, recw) = f32x2{m, xg_sum(l)};
  unsigned sb[12];
#pragma unroll
  for (int y = 0; y < 12; ++y) sb[y] = (fin ? snap : ~snap >> 12) >> y;
  asm volatile("" : "+v"(sb[0]), "+v"(sb[1]), "+v"(sb[2]), "+v"(sb[3]), "+v"(sb[4]), "+v"(sb[5]), "+v"(sb[6]), "+v"(sb[7]), "+v"(sb[8]), "+v"(sb[9]), "+v"(sb[10]), "+v"(sb[11]));
  u32x4 x[NPOLL][3];
#define POLL(k) asm volatile(LOAD3("%0, %3, %6", "%1, %4, %6", "%2, %5, %6")                                        \
                   : "=&v"(x[k][0]), "=&v"(x[k][1]), "=&v"(x[k][2]) : "v"(mb1), "v"(mb2), "v"(mb3), "s"(gws) : "memory")
  WAIT_VM(0);
  V_MMA();
  *LDSP(f32x4, reca) = acc0;
  *LDSP(f32x4, reca + 256) = acc1;
  if (fin) POLL(0);
  f32x2 ml[NW], o[NW];
  asm volatile("s_waitcnt lgkmcnt(0)\n\ts_barrier\n\t"
               "ds_read_b64 %0, %8\n\tds_read_b64 %1, %8 offset:%10\n\tds_read_b64 %2, %8 offset:%11\n\tds_read_b64 %3, %8 offset:%12\n\t"
               "ds_read_b64 %4, %9\n\tds_read_b64 %5, %9 offset:%10\n\tds_read_b64 %6, %9 offset:%11\n\tds_read_b64 %7, %9 offset:%12\n\t"
               "s_waitcnt lgkmcnt(0)"
               : "=&v"(ml[0]), "=&v"(ml[1]), "=&v"(ml[2]), "=&v"(ml[3]), "=&v"(o[0]), "=&v"(o[1]), "=&v"(o[2]), "=&v"(o[3])
               : "v"(recm), "v"(recb), "i"(4 * RED_REC), "i"(8 * RED_REC), "i"(12 * RED_REC) : "memory");
  u32x4 r;
  {
    const float M = __uint_as_float(__float_as_uint(fmaxf(fmaxf(ml[0][0], ml[1][0]), fmaxf(ml[2][0], ml[3][0]))) & ~1u);
    const float a0 = __builtin_amdgcn_exp2f(ml[0][0] - M), a1 = __builtin_amdgcn_exp2f(ml[1][0] - M), a2 = __builtin_amdgcn_exp2f(ml[2][0] - M), a3 = __builtin_amdgcn_exp2f(ml[3][0] - M);
    const float o0 = fmaf(a0, o[0][0], a1 * o[1][0]) + fmaf(a2, o[2][0], a3 * o[3][0]), o1 = fmaf(a0, o[0][1], a1 * o[1][1]) + fmaf(a2, o[2][1], a3 * o[3][1]);
    const float L = fmaf(a0, ml[0][1], a1 * ml[1][1]) + fmaf(a2, ml[2][1], a3 * ml[3][1]);
    r = u32x4{__float_as_uint(o0) & ~1u, __float_as_uint(o1) & ~1u, __float_as_uint(M), __float_as_uint(L) & ~1u};
  }
  if (!fin) {
    const u32x4 u = {r[0] | (sb[0] & 1), r[1] | (sb[1] & 1), r[2] | (sb[2] & 1), r[3] | (sb[3] & 1)};
    __builtin_amdgcn_raw_buffer_store_b128(u, __builtin_amdgcn_make_buffer_rsrc((void*)gws, 0, WS_CTL, 0x00020000), mb0, 0, 16);
  } else {
    POLL(1);
    u32x4 x1, x2, x3;
    asm("" : "=v"(x1), "=v"(x2), "=v"(x3));
    bool done = false;
#pragma unroll 1
    for (unsigned n = 0; n < POLL_CAP && !done; ++n) {
#pragma unroll
      for (int k = 0; k < NPOLL; ++k) {
        asm volatile("s_waitcnt vmcnt(%3)" : "+v"(x[k][0]), "+v"(x[k][1]), "+v"(x[k][2]) : "i"(3 * (NPOLL - 1)) : "memory");
        unsigned fresh = 1;
#pragma unroll
        for (int y = 0; y < 12; ++y) fresh &= x[k][y >> 2][y & 3] ^ sb[y];
        if (__builtin_amdgcn_ballot_w64(fresh == 0) == 0) { x1 = x[k][0]; x2 = x[k][1]; x3 = x[k][2]; done = true; break; }
        POLL(k);
      }
    }
    if (!done) r[3] = ~0u;
    const f32x4 r0 = __builtin_bit_cast(f32x4, r), r1 = __builtin_bit_cast(f32x4, x1 & ~1u), r2 = __builtin_bit_cast(f32x4, x2 & ~1u), r3 = __builtin_bit_cast(f32x4, x3 & ~1u);
    const float M = fmaxf(fmaxf(r0[2], r1[2]), fmaxf(r2[2], r3[2]));
    const float a0 = __builtin_amdgcn_exp2f(r0[2] - M), a1 = __builtin_amdgcn_exp2f(r1[2] - M), a2 = __builtin_amdgcn_exp2f(r2[2] - M), a3 = __builtin_amdgcn_exp2f(r3[2] - M);
    const float L = fmaf(a0, r0[3], a1 * r1[3]) + fmaf(a2, r2[3], a3 * r3[3]);
    const float y0 = fmaf(a0, r0[0], a1 * r1[0]) + fmaf(a2, r2[0], a3 * r3[0]);
    const float y1 = fmaf(a0, r0[1], a1 * r1[1]) + fmaf(a2, r2[1], a3 * r3[1]);
    const float inv = __builtin_amdgcn_rcpf(L);
    *(unsigned*)((char*)out + oo) = cvtpk(y0 * inv, y1 * inv);
#pragma unroll
    for (int k = 0; k < NPOLL; ++k) asm volatile("s_waitcnt vmcnt(0)" : "+v"(x[k][0]), "+v"(x[k][1]), "+v"(x[k][2]) :: "memory");
  }
}

extern "C" void kernel_launch(void* const* d_in, const int* in_sizes, int n_in, void* d_out, int out_size, void* d_ws, size_t ws_size, hipStream_t stream) {
  (void)hipFuncSetAttribute((const void*)gqa_decode, hipFuncAttributeMaxDynamicSharedMemorySize, LDS_BYTES);
  gqa_decode<<<NPAIR * GRP, (NW + 1) * 64, LDS_BYTES, stream>>>((const char*)d_in[1], (const char*)d_in[2], (const char*)d_in[0], (char*)d_ws, (unsigned*)d_out);
}
```

```cpp
#include <hip/hip_runtime.h>
#include <hip/hip_bf16.h>

typedef unsigned u32x4 __attribute__((ext_vector_type(4)));
typedef unsigned u32x2 __attribute__((ext_vector_type(2)));
typedef float f32x4 __attribute__((ext_vector_type(4)));
typedef float f32x2 __attribute__((ext_vector_type(2)));
typedef __bf16 bf16x8 __attribute__((ext_vector_type(8)));
typedef __bf16 bf16x2 __attribute__((ext_vector_type(2)));
#define LDSP(T, a) ((__attribute__((address_space(3))) T*)(size_t)(a))
typedef __attribute__((address_space(1))) unsigned gu32;
#define RLX_AGENT __ATOMIC_RELAXED, __HIP_MEMORY_SCOPE_AGENT

constexpr int SK = 8192, DH = 128, NPAIR = 64, GRP = 4, NW = 4;
constexpr int TILE = 16;
constexpr int NUNIT = SK / (TILE * NW);
constexpr int S_IT = 27;
constexpr int S_SEP = S_IT - 1;
constexpr unsigned NDYN = NUNIT - S_IT * GRP;
constexpr int LEAD = 2;
constexpr int U_SYNC = S_SEP - 8;
constexpr int NPOLL = 2;
static_assert(S_SEP >= 3 && S_SEP <= 28 && NDYN < 60, "score store: 28 tiles + q per wave; ticket table: one LDS word per lane");
constexpr unsigned PAIR_BYTES = SK * DH * 2;
constexpr unsigned TILE_BYTES = TILE * DH * 2;
constexpr unsigned UNIT_BYTES = NW * TILE_BYTES;
constexpr unsigned ROUND_BYTES = GRP * UNIT_BYTES;
constexpr unsigned RING_BYTES = 3 * TILE_BYTES;
constexpr unsigned SC_OFF = NW * RING_BYTES, SC_BYTES = 32 * 256;
constexpr unsigned RED_OFF = SC_OFF + NW * SC_BYTES;
constexpr unsigned RED_REC = (DH + 4) * 4;
constexpr unsigned TK_OFF = RED_OFF + NW * 4 * RED_REC;
constexpr unsigned MSK_OFF = TK_OFF + 256;
constexpr unsigned LDS_BYTES = MSK_OFF + 1024;
static_assert(LDS_BYTES > 80 * 1024 && LDS_BYTES <= 160 * 1024, "exactly one workgroup per CU");
constexpr unsigned G_WS = 32768, WS_CTL = 16384, WS_DIG = WS_CTL + 512;
constexpr unsigned NOTREADY = 0xFFFFFFFFu;
constexpr unsigned BEAT_CAP = 1u << 18, POLL_CAP = 1u << 21;
constexpr float C2 = 0.088388347648318440f * 1.4426950408889634f;

__device__ __forceinline__ unsigned cvtpk(float lo, float hi) {
  bf16x2 v = {(__bf16)lo, (__bf16)hi};
  return __builtin_bit_cast(unsigned, v);
}
__device__ __forceinline__ float xg_max(float x) {
  auto s = __builtin_amdgcn_permlane16_swap(__float_as_uint(x), __float_as_uint(x), false, false);
  x = fmaxf(__uint_as_float(s[0]), __uint_as_float(s[1]));
  auto t = __builtin_amdgcn_permlane32_swap(__float_as_uint(x), __float_as_uint(x), false, false);
  return fmaxf(__uint_as_float(t[0]), __uint_as_float(t[1]));
}
__device__ __forceinline__ float xg_sum(float x) {
  auto s = __builtin_amdgcn_permlane16_swap(__float_as_uint(x), __float_as_uint(x), false, false);
  x = __uint_as_float(s[0]) + __uint_as_float(s[1]);
  auto t = __builtin_amdgcn_permlane32_swap(__float_as_uint(x), __float_as_uint(x), false, false);
  return __uint_as_float(t[0]) + __uint_as_float(t[1]);
}
__device__ __forceinline__ unsigned lsb4(u32x4 x) { return (x[0] & 1) | ((x[1] & 1) << 1) | ((x[2] & 1) << 2) | ((x[3] & 1) << 3); }

#define DMA_LOADS "buffer_load_dwordx4 %3, %1, %2 offen nt lds\n\tbuffer_load_dwordx4 %4, %1, %2 offen offset:1024 nt lds\n\t" \
                  "buffer_load_dwordx4 %5, %1, %2 offen offset:2048 nt lds\n\tbuffer_load_dwordx4 %6, %1, %2 offen offset:3072 nt lds"
#define DMA_ARGS(p, m0v, soff) "s"(m0v), "s"(__builtin_amdgcn_make_buffer_rsrc((void*)(p), 0, NPAIR * PAIR_BYTES, 0x00020000)), "s"(soff), \
                               "v"(so0), "v"(so1), "v"(so2), "v"(so3)
#define DMA_TILE(p, m0v, soff) asm volatile("s_mov_b32 m0, %0\n\ts_nop 0\n\t" DMA_LOADS :: DMA_ARGS(p, m0v, soff) : "memory")
#define WAIT_VM(n) asm volatile("s_waitcnt vmcnt(" #n ")" ::: "memory")
#define LOAD3(x1, x2, x3) "global_load_dwordx4 " x1 " sc1\n\tglobal_load_dwordx4 " x2 " sc1\n\tglobal_load_dwordx4 " x3 " sc1\n\t"

__global__ void __launch_bounds__(320, 2) gqa_decode(const char* __restrict__ K, const char* __restrict__ V, const char* __restrict__ Q, char* ws, unsigned* __restrict__ out) {
  extern __shared__ __attribute__((aligned(256))) char lds[];
  unsigned early;
  {
    unsigned long long pc;
    asm volatile("s_getpc_b64 %1\n\tv_mov_b32 %0, 0\n\tglobal_load_dword %0, %0, %1" : "=&v"(early), "=&s"(pc) :: "memory");
  }
  __builtin_amdgcn_sched_barrier(0);
  const unsigned bid = blockIdx.x, pair = (bid >> 3) * 2 + (((bid >> 1) ^ (bid & (bid >> 3))) & 1), j = (bid & 1) | ((bid >> 1) & 2);
  const unsigned lane = threadIdx.x & 63, w = __builtin_amdgcn_readfirstlane(threadIdx.x >> 6);
  const unsigned g = lane >> 4, c = lane & 15;
  unsigned so0 = g * 256 + ((c ^ (g << 2)) << 4), so1 = (so0 ^ 32) + 3072, so2 = (so0 ^ 48) + 2 * 3072, so3 = (so0 ^ 16) + 3 * 3072;
  unsigned qo = lane * 16;
  const unsigned ldsb = (unsigned)(size_t)lds;
  const unsigned ring = ldsb + w * RING_BYTES;
  const unsigned woff = pair * PAIR_BYTES + w * 1024, soff0 = woff + j * UNIT_BYTES;
  asm volatile("" : "+v"(so0), "+v"(so1), "+v"(so2), "+v"(so3), "+v"(qo) : "s"(ring), "s"(soff0));
  __builtin_amdgcn_sched_barrier(0);
  asm volatile("s_waitcnt vmcnt(0)" : "+v"(early) :: "memory");
  asm volatile("" :: "s"(K), "s"(V), "s"(Q), "s"(ws), "s"(out));
  asm volatile("s_mov_b32 m0, %0\n\ts_cmp_eq_u32 %7, %8\n\ts_cbranch_scc1 1f\n\t" DMA_LOADS "\n1:"
               :: DMA_ARGS(K, ring, soff0), "s"(w), "i"(NW) : "memory", "scc");
  __builtin_amdgcn_sched_barrier(0);
  char* const gws = ws + (size_t)pair * G_WS;

  if (w == NW) {
    volatile __attribute__((address_space(3))) unsigned* const tk = LDSP(volatile unsigned, ldsb + TK_OFF);
    __builtin_amdgcn_s_sleep(64);
    tk[lane] = lane == 63 ? 0u : NOTREADY;
    __builtin_amdgcn_s_setprio(3);
    gu32* const ctl = (gu32*)(gws + WS_CTL);
    const unsigned ma = ldsb + MSK_OFF;
    unsigned e0; u32x4 a0, a1, a2, a3;
    asm volatile("global_load_dword %0, %5, %7 sc1\n\tglobal_load_dwordx4 %1, %6, %7 sc1\n\t"
                 LOAD3("%2, %6, %7 offset:1024", "%3, %6, %7 offset:2048", "%4, %6, %7 offset:3072") "s_waitcnt vmcnt(0)"
                 : "=&v"(e0), "=&v"(a0), "=&v"(a1), "=&v"(a2), "=&v"(a3) : "v"(WS_CTL + (lane & 7) * 4), "v"(lane * 16 + j * 4096), "s"(gws) : "memory");
    const unsigned c0 = __builtin_amdgcn_readlane(e0, 4);
    const unsigned d0 = lsb4(a0) | (lsb4(a1) << 4) | (lsb4(a2) << 8) | (lsb4(a3) << 12);
    *LDSP(volatile unsigned short, ma + lane * 2) = (unsigned short)d0;
    const u32x4 dg = *LDSP(volatile u32x4, ma + (lane & 7) * 16);
    __builtin_amdgcn_raw_buffer_store_b128(dg, __builtin_amdgcn_make_buffer_rsrc((void*)(gws + WS_DIG + j * 128), 0, 128, 0x00020000), lane * 16, 0, 16);
    WAIT_VM(0);
    if (lane == 0) __hip_atomic_fetch_add(ctl + j, 1u, RLX_AGENT);
    bool fail = true;
#pragma unroll 1
    for (unsigned n = 0; n < BEAT_CAP; ++n) {
      __builtin_amdgcn_s_sleep(64);
      const unsigned e = __hip_atomic_load(ctl + (lane & 7), RLX_AGENT);
      if (__builtin_amdgcn_ballot_w64(lane < 4 && lane != j && e == e0) == 0) { fail = false; break; }
    }
    if (lane == 0) __hip_atomic_fetch_add(ctl + j, 1u, RLX_AGENT);
    unsigned d1, d2, d3;
    asm volatile("global_load_dword %0, %3, %6 sc1\n\tglobal_load_dword %1, %4, %6 sc1\n\tglobal_load_dword %2, %5, %6 sc1\n\ts_waitcnt vmcnt(0)"
                 : "=&v"(d1), "=&v"(d2), "=&v"(d3)
                 : "v"(WS_DIG + (j ^ 1) * 128 + (lane >> 1) * 4), "v"(WS_DIG + (j ^ 2) * 128 + (lane >> 1) * 4), "v"(WS_DIG + (j ^ 3) * 128 + (lane >> 1) * 4), "s"(gws) : "memory");
    d1 >>= (lane & 1) * 16; d2 >>= (lane & 1) * 16; d3 >>= (lane & 1) * 16;
#pragma unroll
    for (int y = 0; y < NW; ++y)
      *LDSP(volatile unsigned, ma + y * 256 + lane * 4) = ((d1 >> 4 * y) & 15) | (((d2 >> 4 * y) & 15) << 4) | (((d3 >> 4 * y) & 15) << 8) | (((d0 >> 4 * y) & 15) << 12);
    if (fail) out[(pair * 4 + j) * 64 + lane] = ~0u;
    asm volatile("s_waitcnt lgkmcnt(0)\n\ts_barrier" ::: "memory");
#pragma unroll 1
    for (unsigned k = 0;; ++k) {
      while (__builtin_amdgcn_readfirstlane(tk[63]) < 2 * (S_IT - 1) - LEAD + 2 * k) __builtin_amdgcn_s_sleep(1);
      unsigned t = NDYN;
      if (!fail) {
        unsigned tv = 0;
        if (lane == 0) tv = __hip_atomic_fetch_add(ctl + 4, 1u, RLX_AGENT);
        t = min(__builtin_amdgcn_readfirstlane(tv) - c0, NDYN);
        if (t == NDYN - 1) tk[k++] = t++;
      }
      tk[k] = t;
      if (t == NDYN) break;
    }
    asm volatile("s_waitcnt lgkmcnt(0)\n\ts_barrier" ::: "memory");
    return;
  }

  const unsigned qst = ldsb + SC_OFF + (w + 1) * SC_BYTES - 1024;
  asm volatile("s_mov_b32 m0, %0\n\ts_nop 0\n\tglobal_load_lds_dwordx4 %1, %2" :: "s"(qst), "v"(qo), "s"(Q + (size_t)pair * 1024) : "memory");
  DMA_TILE(K, ring + TILE_BYTES, soff0 + ROUND_BYTES);
  DMA_TILE(K, ring + 2 * TILE_BYTES, soff0 + 2 * ROUND_BYTES);

  const unsigned fc = ((c & 3) << 2) | ((((c >> 2) ^ (c >> 3)) & 1) << 1) | (c >> 3);
  const unsigned ka = c * 256 + ((g ^ fc) << 4);
  const unsigned qq = (lane >> 2) & 3, pp = lane & 3;
  const unsigned fr = (qq << 2) | (((g ^ (g >> 1)) & 1) << 1) | (g >> 1);
  const unsigned va = (4 * g + qq) * 256 + (((pp >> 1) ^ fr) << 4) + 8 * (pp & 1);
  const unsigned sa = ldsb + SC_OFF + w * SC_BYTES + (c & 3) * 64 + g * 16;
  float m = -INFINITY, mrun = -INFINITY, l = 0.f;
  unsigned rd = ring, wr = ring;
  u32x4 q0, q1, q2, q3;
  asm volatile("s_waitcnt vmcnt(8)\n\tds_read_b128 %0, %4\n\tds_read_b128 %1, %4 offset:64\n\tds_read_b128 %2, %4 offset:128\n\t"
               "ds_read_b128 %3, %4 offset:192\n\ts_waitcnt lgkmcnt(0)"
               : "=&v"(q0), "=&v"(q1), "=&v"(q2), "=&v"(q3) : "v"(qst + (c & 3) * 256 + g * 16) : "memory");

#define ADVANCE() do { wr = rd; rd += TILE_BYTES; if (rd == ring + RING_BYTES) rd = ring; } while (0)
#define PROGRESS(h) asm volatile("s_mov_b64 exec, 1\n\tds_max_u32 %0, %1\n\ts_mov_b64 exec, -1" :: "v"(ldsb + TK_OFF + 252), "v"(h) : "memory")
#define K_ADDR() unsigned a0 = rd + ka, a1 = a0 ^ 64, a2 = a0 ^ 128, a3 = a0 ^ 192; asm volatile("" : "+v"(a0), "+v"(a1), "+v"(a2), "+v"(a3))
#define K_STEP(sc) do {                    \
    u32x4 k0, k1, k2, k3;                                                                                           \
    asm volatile("ds_read_b128 %0, %4\n\tds_read_b128 %1, %5\n\tds_read_b128 %2, %6\n\tds_read_b128 %3, %7\n\ts_waitcnt lgkmcnt(0)" \
                 : "=&v"(k0), "=&v"(k1), "=&v"(k2), "=&v"(k3) : "v"(a0), "v"(a1), "v"(a2), "v"(a3) : "memory");      \
    f32x4 s = {0.f, 0.f, 0.f, 0.f};                                                                                 \
    s = __builtin_amdgcn_mfma_f32_16x16x32_bf16(__builtin_bit_cast(bf16x8, k0), __builtin_bit_cast(bf16x8, q0), s, 0, 0, 0); \
    s = __builtin_amdgcn_mfma_f32_16x16x32_bf16(__builtin_bit_cast(bf16x8, k1), __builtin_bit_cast(bf16x8, q1), s, 0, 0, 0); \
    s = __builtin_amdgcn_mfma_f32_16x16x32_bf16(__builtin_bit_cast(bf16x8, k2), __builtin_bit_cast(bf16x8, q2), s, 0, 0, 0); \
    s = __builtin_amdgcn_mfma_f32_16x16x32_bf16(__builtin_bit_cast(bf16x8, k3), __builtin_bit_cast(bf16x8, q3), s, 0, 0, 0); \
    mrun = fmaxf(mrun, fmaxf(fmaxf(s[0], s[1]), fmaxf(s[2], s[3])) * C2);                                           \
    *LDSP(f32x4, sc) = s;                                                                                           \
    ADVANCE();                                                                                                      \
  } while (0)

#pragma unroll 1
  for (int h = 0; h < S_SEP; ++h) {
    K_ADDR();
    if (h != 0) {
      const bool nv = h + 2 >= S_SEP;
      DMA_TILE(nv ? V : K, wr, soff0 + (unsigned)(nv ? h + 2 - S_SEP : h + 2) * ROUND_BYTES);
      WAIT_VM(8);
    }
    K_STEP(sa + 256 * h);
  }
  f32x4 acc0 = {0.f, 0.f, 0.f, 0.f}, acc1 = acc0;
  unsigned reca = ldsb + RED_OFF + (w * 4 + (c & 3)) * RED_REC + (c >> 2) * 64 + g * 16;
  unsigned recw = ldsb + RED_OFF + (w * 4 + (c & 3)) * RED_REC + DH * 4;
  unsigned recb = ldsb + RED_OFF + w * RED_REC + lane * 8;
  unsigned recm = ldsb + RED_OFF + w * RED_REC + DH * 4;
  unsigned mb0 = lane * 16 + j * 4096 + w * 1024, mb1 = mb0 ^ 4096, mb2 = mb0 ^ 8192, mb3 = mb0 ^ 12288;
  unsigned oo = ((pair * 4 + w) * 64 + lane) * 4;
  asm volatile("" : "+v"(reca), "+v"(recw), "+v"(recb), "+v"(recm), "+v"(mb0), "+v"(mb1), "+v"(mb2), "+v"(mb3), "+v"(oo));
  unsigned snap = 0;
  unsigned noff = 0;
  bool more = true, fin = false;
  u32x2 pk0, pk1, pk2, pk3; unsigned b0, b1, b2, b3, b4, b5, b6, b7;
#define PV(acc, va_, vb_, pa_, pb_) acc = __builtin_amdgcn_mfma_f32_16x16x32_bf16(__builtin_bit_cast(bf16x8, __builtin_shufflevector(va_, vb_, 0, 1, 2, 3)), \
                                                                            __builtin_bit_cast(bf16x8, __builtin_shufflevector(pa_, pb_, 0, 1, 2, 3)), acc, 0, 0, 0)
#define V_MMA() do {                                                                                                \
    u32x2 v0, v1, v2, v3, v4, v5, v6, v7;                                                                           \
    asm volatile("ds_read_b64_tr_b16 %0, %8\n\tds_read_b64_tr_b16 %1, %9\n\tds_read_b64_tr_b16 %2, %10\n\tds_read_b64_tr_b16 %3, %11\n\t" \
                 "ds_read_b64_tr_b16 %4, %12\n\tds_read_b64_tr_b16 %5, %13\n\tds_read_b64_tr_b16 %6, %14\n\tds_read_b64_tr_b16 %7, %15\n\t" \
                 "s_waitcnt lgkmcnt(0)"                                                                             \
                 : "=&v"(v0), "=&v"(v1), "=&v"(v2), "=&v"(v3), "=&v"(v4), "=&v"(v5), "=&v"(v6), "=&v"(v7)            \
                 : "v"(b0), "v"(b1), "v"(b2), "v"(b3), "v"(b4), "v"(b5), "v"(b6), "v"(b7) : "memory");               \
    PV(acc0, v0, v1, pk0, pk1); PV(acc1, v4, v5, pk0, pk1); PV(acc0, v2, v3, pk2, pk3); PV(acc1, v6, v7, pk2, pk3); \
  } while (0)
#pragma unroll 1
  for (int u = 0;; ++u) {
    if (u >= S_SEP) {
      K_ADDR();
      PROGRESS(2 * u);
      unsigned t;
      do t = __builtin_amdgcn_readfirstlane(*LDSP(volatile unsigned, ldsb + TK_OFF + 4 * (u - S_SEP))); while (t == NOTREADY);
      more = t < NDYN;
      fin |= t == NDYN - 1;
      noff = woff + (S_IT * GRP + t) * UNIT_BYTES;
      if (more) {
        DMA_TILE(K, wr, noff);
        WAIT_VM(8);
      } else {
        snap = *LDSP(volatile unsigned, ldsb + MSK_OFF + (w * 64 + lane) * 4);
        WAIT_VM(4);
      }
      K_STEP(sa);
    }
    if (u > U_SYNC) PROGRESS(u < S_SEP ? S_SEP + u : 2 * u + 1);
    b0 = rd + va; b1 = b0 ^ 32; b2 = b0 ^ 64; b3 = b0 ^ 96; b4 = b0 ^ 128; b5 = b0 ^ 160; b6 = b0 ^ 192; b7 = b0 ^ 224;
    if (__builtin_amdgcn_ballot_w64(mrun > m) != 0) {
      const float mn = xg_max(mrun);
      const float al = __builtin_amdgcn_exp2f(m - mn);
      m = mn; l *= al; acc0 *= al; acc1 *= al;
    }
    const f32x4 s = *LDSP(const f32x4, sa + 256 * (unsigned)(u < S_SEP ? u : 0));
    const float p0 = __builtin_amdgcn_exp2f(fmaf(s[0], C2, -m)), p1 = __builtin_amdgcn_exp2f(fmaf(s[1], C2, -m));
    const float p2 = __builtin_amdgcn_exp2f(fmaf(s[2], C2, -m)), p3 = __builtin_amdgcn_exp2f(fmaf(s[3], C2, -m));
    l += (p0 + p1) + (p2 + p3);
    const u32x2 pk = {cvtpk(p0, p1), cvtpk(p2, p3)}, pz = {0u, 0u};
    pk0 = (c >> 2) == 0 ? pk : pz; pk1 = (c >> 2) == 1 ? pk : pz; pk2 = (c >> 2) == 2 ? pk : pz; pk3 = (c >> 2) == 3 ? pk : pz;
    asm volatile("" : "+v"(pk0), "+v"(pk1), "+v"(pk2), "+v"(pk3), "+v"(b0), "+v"(b1), "+v"(b2), "+v"(b3), "+v"(b4), "+v"(b5), "+v"(b6), "+v"(b7));
    if (!more) break;
    DMA_TILE(u == S_SEP - 2 ? K : V, wr, u >= S_SEP ? noff : soff0 + (unsigned)min(u + 2, S_SEP) * ROUND_BYTES);
    if (u == U_SYNC) __builtin_amdgcn_s_barrier();
    WAIT_VM(8);
    V_MMA();
    ADVANCE();
  }

  *LDSP(f32x2, recw) = f32x2{m, xg_sum(l)};
  unsigned sb[12];
#pragma unroll
  for (int y = 0; y < 12; ++y) sb[y] = (fin ? snap : ~snap >> 12) >> y;
  asm volatile("" : "+v"(sb[0]), "+v"(sb[1]), "+v"(sb[2]), "+v"(sb[3]), "+v"(sb[4]), "+v"(sb[5]), "+v"(sb[6]), "+v"(sb[7]), "+v"(sb[8]), "+v"(sb[9]), "+v"(sb[10]), "+v"(sb[11]));
  u32x4 x[NPOLL][3];
#define POLL(k) asm volatile(LOAD3("%0, %3, %6", "%1, %4, %6", "%2, %5, %6")                                        \
                   : "=&v"(x[k][0]), "=&v"(x[k][1]), "=&v"(x[k][2]) : "v"(mb1), "v"(mb2), "v"(mb3), "s"(gws) : "memory")
  WAIT_VM(0);
  V_MMA();
  *LDSP(f32x4, reca) = acc0;
  *LDSP(f32x4, reca + 256) = acc1;
  if (fin) POLL(0);
  f32x2 ml[NW], o[NW];
  asm volatile("s_waitcnt lgkmcnt(0)\n\ts_barrier\n\t"
               "ds_read_b64 %0, %8\n\tds_read_b64 %1, %8 offset:%10\n\tds_read_b64 %2, %8 offset:%11\n\tds_read_b64 %3, %8 offset:%12\n\t"
               "ds_read_b64 %4, %9\n\tds_read_b64 %5, %9 offset:%10\n\tds_read_b64 %6, %9 offset:%11\n\tds_read_b64 %7, %9 offset:%12\n\t"
               "s_waitcnt lgkmcnt(0)"
               : "=&v"(ml[0]), "=&v"(ml[1]), "=&v"(ml[2]), "=&v"(ml[3]), "=&v"(o[0]), "=&v"(o[1]), "=&v"(o[2]), "=&v"(o[3])
               : "v"(recm), "v"(recb), "i"(4 * RED_REC), "i"(8 * RED_REC), "i"(12 * RED_REC) : "memory");
  u32x4 r;
  {
    const float M = __uint_as_float(__float_as_uint(fmaxf(fmaxf(ml[0][0], ml[1][0]), fmaxf(ml[2][0], ml[3][0]))) & ~1u);
    const float a0 = __builtin_amdgcn_exp2f(ml[0][0] - M), a1 = __builtin_amdgcn_exp2f(ml[1][0] - M), a2 = __builtin_amdgcn_exp2f(ml[2][0] - M), a3 = __builtin_amdgcn_exp2f(ml[3][0] - M);
    const float o0 = fmaf(a0, o[0][0], a1 * o[1][0]) + fmaf(a2, o[2][0], a3 * o[3][0]), o1 = fmaf(a0, o[0][1], a1 * o[1][1]) + fmaf(a2, o[2][1], a3 * o[3][1]);
    const float L = fmaf(a0, ml[0][1], a1 * ml[1][1]) + fmaf(a2, ml[2][1], a3 * ml[3][1]);
    r = u32x4{__float_as_uint(o0) & ~1u, __float_as_uint(o1) & ~1u, __float_as_uint(M), __float_as_uint(L) & ~1u};
  }
  if (!fin) {
    const u32x4 u = {r[0] | (sb[0] & 1), r[1] | (sb[1] & 1), r[2] | (sb[2] & 1), r[3] | (sb[3] & 1)};
    __builtin_amdgcn_raw_buffer_store_b128(u, __builtin_amdgcn_make_buffer_rsrc((void*)gws, 0, WS_CTL, 0x00020000), mb0, 0, 16);
  } else {
    POLL(1);
    u32x4 x1, x2, x3;
    asm("" : "=v"(x1), "=v"(x2), "=v"(x3));
    bool done = false;
#pragma unroll 1
    for (unsigned n = 0; n < POLL_CAP && !done; ++n) {
#pragma unroll
      for (int k = 0; k < NPOLL; ++k) {
        asm volatile("s_waitcnt vmcnt(%3)" : "+v"(x[k][0]), "+v"(x[k][1]), "+v"(x[k][2]) : "i"(3 * (NPOLL - 1)) : "memory");
        unsigned fresh = 1;
#pragma unroll
        for (int y = 0; y < 12; ++y) fresh &= x[k][y >> 2][y & 3] ^ sb[y];
        if (__builtin_amdgcn_ballot_w64(fresh == 0) == 0) { x1 = x[k][0]; x2 = x[k][1]; x3 = x[k][2]; done = true; break; }
        POLL(k);
      }
    }
    if (!done) r[3] = ~0u;
    const f32x4 r0 = __builtin_bit_cast(f32x4, r), r1 = __builtin_bit_cast(f32x4, x1 & ~1u), r2 = __builtin_bit_cast(f32x4, x2 & ~1u), r3 = __builtin_bit_cast(f32x4, x3 & ~1u);
    const float M = fmaxf(fmaxf(r0[2], r1[2]), fmaxf(r2[2], r3[2]));
    const float a0 = __builtin_amdgcn_exp2f(r0[2] - M), a1 = __builtin_amdgcn_exp2f(r1[2] - M), a2 = __builtin_amdgcn_exp2f(r2[2] - M), a3 = __builtin_amdgcn_exp2f(r3[2] - M);
    const float L = fmaf(a0, r0[3], a1 * r1[3]) + fmaf(a2, r2[3], a3 * r3[3]);
    const float y0 = fmaf(a0, r0[0], a1 * r1[0]) + fmaf(a2, r2[0], a3 * r3[0]);
    const float y1 = fmaf(a0, r0[1], a1 * r1[1]) + fmaf(a2, r2[1], a3 * r3[1]);
    const float inv = __builtin_amdgcn_rcpf(L);
    *(unsigned*)((char*)out + oo) = cvtpk(y0 * inv, y1 * inv);
#pragma unroll
    for (int k = 0; k < NPOLL; ++k) asm volatile("s_waitcnt vmcnt(0)" : "+v"(x[k][0]), "+v"(x[k][1]), "+v"(x[k][2]) :: "memory");
  }
}

extern "C" void kernel_launch(void* const* d_in, const int* in_sizes, int n_in, void* d_out, int out_size, void* d_ws, size_t ws_size, hipStream_t stream) {
  (void)hipFuncSetAttribute((const void*)gqa_decode, hipFuncAttributeMaxDynamicSharedMemorySize, LDS_BYTES);
  gqa_decode<<<NPAIR * GRP, (NW + 1) * 64, LDS_BYTES, stream>>>((const char*)d_in[1], (const char*)d_in[2], (const char*)d_in[0], (char*)d_ws, (unsigned*)d_out);
}
```

```cpp
#include <hip/hip_runtime.h>
#include <hip/hip_bf16.h>

typedef unsigned u32x4 __attribute__((ext_vector_type(4)));
typedef unsigned u32x2 __attribute__((ext_vector_type(2)));
typedef float f32x4 __attribute__((ext_vector_type(4)));
typedef float f32x2 __attribute__((ext_vector_type(2)));
typedef __bf16 bf16x8 __attribute__((ext_vector_type(8)));
typedef __bf16 bf16x2 __attribute__((ext_vector_type(2)));
#define LDSP(T, a) ((__attribute__((address_space(3))) T*)(size_t)(a))
typedef __attribute__((address_space(1))) unsigned gu32;
#define RLX_AGENT __ATOMIC_RELAXED, __HIP_MEMORY_SCOPE_AGENT

constexpr int SK = 8192, DH = 128, NPAIR = 64, GRP = 4, NW = 4;
constexpr int TILE = 16;
constexpr int NUNIT = SK / (TILE * NW);
constexpr int S_IT = 27;
constexpr int S_SEP = S_IT - 1;
constexpr unsigned NDYN = NUNIT - S_IT * GRP;
constexpr int LEAD = 2;
constexpr int U_SYNC = S_SEP - 8;
constexpr int NPOLL = 2;
static_assert(S_SEP >= 3 && S_SEP <= 28 && NDYN < 60, "score store: 28 tiles + q per wave; ticket table: one LDS word per lane");
constexpr unsigned PAIR_BYTES = SK * DH * 2;
constexpr unsigned TILE_BYTES = TILE * DH * 2;
constexpr unsigned UNIT_BYTES = NW * TILE_BYTES;
constexpr unsigned ROUND_BYTES = GRP * UNIT_BYTES;
constexpr unsigned RING_BYTES = 3 * TILE_BYTES;
constexpr unsigned SC_OFF = NW * RING_BYTES, SC_BYTES = 32 * 256;
constexpr unsigned RED_OFF = SC_OFF + NW * SC_BYTES;
constexpr unsigned RED_REC = (DH + 4) * 4;
constexpr unsigned TK_OFF = RED_OFF + NW * 4 * RED_REC;
constexpr unsigned MSK_OFF = TK_OFF + 256;
constexpr unsigned LDS_BYTES = MSK_OFF + 1024 + 256;
static_assert(LDS_BYTES > 80 * 1024 && LDS_BYTES <= 160 * 1024, "exactly one workgroup per CU");
constexpr unsigned G_WS = 32768, WS_CTL = 16384, WS_DIG = WS_CTL + 512;
constexpr unsigned NOTREADY = 0xFFFFFFFFu;
constexpr unsigned BEAT_CAP = 1u << 18, POLL_CAP = 1u << 21;
constexpr float C2 = 0.088388347648318440f * 1.4426950408889634f;

__device__ __forceinline__ unsigned cvtpk(float lo, float hi) {
  bf16x2 v = {(__bf16)lo, (__bf16)hi};
  return __builtin_bit_cast(unsigned, v);
}
__device__ __forceinline__ float xg_max(float x) {
  auto s = __builtin_amdgcn_permlane16_swap(__float_as_uint(x), __float_as_uint(x), false, false);
  x = fmaxf(__uint_as_float(s[0]), __uint_as_float(s[1]));
  auto t = __builtin_amdgcn_permlane32_swap(__float_as_uint(x), __float_as_uint(x), false, false);
  return fmaxf(__uint_as_float(t[0]), __uint_as_float(t[1]));
}
__device__ __forceinline__ float xg_sum(float x) {
  auto s = __builtin_amdgcn_permlane16_swap(__float_as_uint(x), __float_as_uint(x), false, false);
  x = __uint_as_float(s[0]) + __uint_as_float(s[1]);
  auto t = __builtin_amdgcn_permlane32_swap(__float_as_uint(x), __float_as_uint(x), false, false);
  return __uint_as_float(t[0]) + __uint_as_float(t[1]);
}
__device__ __forceinline__ unsigned lsb4(u32x4 x) { return (x[0] & 1) | ((x[1] & 1) << 1) | ((x[2] & 1) << 2) | ((x[3] & 1) << 3); }

#define DMA_LOADS "buffer_load_dwordx4 %3, %1, %2 offen nt lds\n\tbuffer_load_dwordx4 %4, %1, %2 offen offset:1024 nt lds\n\t" \
                  "buffer_load_dwordx4 %5, %1, %2 offen offset:2048 nt lds\n\tbuffer_load_dwordx4 %6, %1, %2 offen offset:3072 nt lds"
#define DMA_ARGS(p, m0v, soff) "s"(m0v), "s"(__builtin_amdgcn_make_buffer_rsrc((void*)(p), 0, NPAIR * PAIR_BYTES, 0x00020000)), "s"(soff), \
                               "v"(so0), "v"(so1), "v"(so2), "v"(so3)
#define DMA_TILE(p, m0v, soff) asm volatile("s_mov_b32 m0, %0\n\ts_nop 0\n\t" DMA_LOADS :: DMA_ARGS(p, m0v, soff) : "memory")
#define WAIT_VM(n) asm volatile("s_waitcnt vmcnt(" #n ")" ::: "memory")
#define LOAD2(x1, x2) "global_load_dwordx4 " x1 " sc1\n\tglobal_load_dwordx4 " x2 " sc1\n\t"

__global__ void __launch_bounds__(320, 2) gqa_decode(const char* __restrict__ K, const char* __restrict__ V, const char* __restrict__ Q, char* ws, unsigned* __restrict__ out) {
  extern __shared__ __attribute__((aligned(256))) char lds[];
  unsigned pfd;
  {
    unsigned long long pc;
    asm volatile("s_getpc_b64 %0" : "=s"(pc));
    asm volatile("global_load_dword %0, %1, %2" : "=v"(pfd) : "v"(min(threadIdx.x * 16, 4864u)), "s"(pc) : "memory");
  }
  const unsigned bid = blockIdx.x, pair = (bid >> 3) * 2 + (((bid >> 1) ^ (bid & (bid >> 3))) & 1), j = (bid & 1) | ((bid >> 1) & 2);
  const unsigned lane = threadIdx.x & 63, w = __builtin_amdgcn_readfirstlane(threadIdx.x >> 6);
  const unsigned g = lane >> 4, c = lane & 15;
  unsigned so0 = g * 256 + ((c ^ (g << 2)) << 4), so1 = (so0 ^ 32) + 3072, so2 = (so0 ^ 48) + 2 * 3072, so3 = (so0 ^ 16) + 3 * 3072;
  unsigned qo = lane * 16;
  const unsigned ldsb = (unsigned)(size_t)lds;
  const unsigned ring = ldsb + w * RING_BYTES;
  const unsigned woff = pair * PAIR_BYTES + w * 1024, soff0 = woff + j * UNIT_BYTES;
  asm volatile("" : "+v"(so0), "+v"(so1), "+v"(so2), "+v"(so3), "+v"(qo) : "s"(ring), "s"(soff0));
  __builtin_amdgcn_sched_barrier(0);
  asm volatile("" :: "s"(K), "s"(V), "s"(Q), "s"(ws), "s"(out));
  asm volatile("s_mov_b32 m0, %0\n\ts_cmp_eq_u32 %7, %8\n\ts_cbranch_scc1 1f\n\t" DMA_LOADS "\n1:"
               :: DMA_ARGS(K, ring, soff0), "s"(w), "i"(NW) : "memory", "scc");
  __builtin_amdgcn_sched_barrier(0);
  char* const gws = ws + (size_t)pair * G_WS;

  if (w == NW) {
    volatile __attribute__((address_space(3))) unsigned* const tk = LDSP(volatile unsigned, ldsb + TK_OFF);
    for (unsigned n = 0; n < 6 + (pair >> 1); ++n) __builtin_amdgcn_s_sleep(10);
    tk[lane] = lane == 63 ? 0u : NOTREADY;
    __builtin_amdgcn_s_setprio(3);
    asm volatile("s_waitcnt vmcnt(0)" : "+v"(pfd) :: "memory");
    gu32* const ctl = (gu32*)(gws + WS_CTL);
    const unsigned ma = ldsb + MSK_OFF;
    unsigned e0; u32x4 a0, a1;
    asm volatile("global_load_dword %0, %3, %5 sc1\n\t" LOAD2("%1, %4, %5", "%2, %6, %5") "s_waitcnt vmcnt(0)"
                 : "=&v"(e0), "=&v"(a0), "=&v"(a1) : "v"(WS_CTL + (lane & 7) * 4), "v"((lane & 31) * 16 + j * 512 + (lane >> 5) * 4096), "s"(gws),
                   "v"((lane & 31) * 16 + j * 512 + (lane >> 5) * 4096 + 8192) : "memory");
    const unsigned c0 = __builtin_amdgcn_readlane(e0, 4);
    *LDSP(volatile unsigned char, ma + 1024 + lane) = (unsigned char)(lsb4(a0) | (lsb4(a1) << 4));
    const u32x4 dg = *LDSP(volatile u32x4, ma + 1024 + (lane & 3) * 16);
    __builtin_amdgcn_raw_buffer_store_b128(dg, __builtin_amdgcn_make_buffer_rsrc((void*)(gws + WS_DIG + j * 128), 0, 128, 0x00020000), lane * 16, 0, 16);
    WAIT_VM(0);
    if (lane == 0) __hip_atomic_fetch_add(ctl + j, 1u, RLX_AGENT);
    bool fail = true;
#pragma unroll 1
    for (unsigned n = 0; n < BEAT_CAP; ++n) {
      __builtin_amdgcn_s_sleep(64);
      const unsigned e = __hip_atomic_load(ctl + (lane & 7), RLX_AGENT);
      if (__builtin_amdgcn_ballot_w64(lane < 4 && lane != j && e == e0) == 0) { fail = false; break; }
    }
    if (lane == 0) __hip_atomic_fetch_add(ctl + j, 1u, RLX_AGENT);
    unsigned dd;
    asm volatile("global_load_dword %0, %1, %2 sc1\n\ts_waitcnt vmcnt(0)" : "=&v"(dd) : "v"(WS_DIG + (j ^ (lane >> 4)) * 128 + (lane & 15) * 4), "s"(gws) : "memory");
    *LDSP(volatile unsigned, ma + 1024 + lane * 4) = dd;
#pragma unroll
    for (int y = 0; y < NW; ++y) {
      const unsigned db = ma + 1024 + (y & 1) * 32 + (lane & 31);
      const unsigned n0 = *LDSP(volatile unsigned char, db), n1 = *LDSP(volatile unsigned char, db + 64 + (lane & 32) * 2), n3 = *LDSP(volatile unsigned char, db + 192);
      *LDSP(volatile unsigned, ma + y * 256 + lane * 4) = ((n1 >> 4 * (y >> 1)) & 15) | (((n3 >> 4 * (y >> 1)) & 15) << 4) | (((n0 >> 4 * (y >> 1)) & 15) << 8);
    }
    if (fail) out[(pair * 4 + j) * 64 + lane] = ~0u;
    asm volatile("s_waitcnt lgkmcnt(0)\n\ts_barrier" ::: "memory");
#pragma unroll 1
    for (unsigned k = 0;; ++k) {
      while (__builtin_amdgcn_readfirstlane(tk[63]) < 2 * (S_IT - 1) - LEAD + 2 * k) __builtin_amdgcn_s_sleep(1);
      unsigned t = NDYN;
      if (!fail) {
        unsigned tv = 0;
        if (lane == 0) tv = __hip_atomic_fetch_add(ctl + 4, 1u, RLX_AGENT);
        t = min(__builtin_amdgcn_readfirstlane(tv) - c0, NDYN);
        if (t == NDYN - 1) tk[k++] = t++;
      }
      tk[k] = t;
      if (t == NDYN) break;
    }
    asm volatile("s_waitcnt lgkmcnt(0)\n\ts_barrier" ::: "memory");
    return;
  }

  const unsigned qst = ldsb + SC_OFF + (w + 1) * SC_BYTES - 1024;
  asm volatile("s_mov_b32 m0, %0\n\ts_nop 0\n\tglobal_load_lds_dwordx4 %1, %2" :: "s"(qst), "v"(qo), "s"(Q + (size_t)pair * 1024) : "memory");
  DMA_TILE(K, ring + TILE_BYTES, soff0 + ROUND_BYTES);
  DMA_TILE(K, ring + 2 * TILE_BYTES, soff0 + 2 * ROUND_BYTES);

  const unsigned fc = ((c & 3) << 2) | ((((c >> 2) ^ (c >> 3)) & 1) << 1) | (c >> 3);
  const unsigned ka = c * 256 + ((g ^ fc) << 4);
  const unsigned qq = (lane >> 2) & 3, pp = lane & 3;
  const unsigned fr = (qq << 2) | (((g ^ (g >> 1)) & 1) << 1) | (g >> 1);
  const unsigned va = (4 * g + qq) * 256 + (((pp >> 1) ^ fr) << 4) + 8 * (pp & 1);
  const unsigned sa = ldsb + SC_OFF + w * SC_BYTES + (c & 3) * 64 + g * 16;
  float m = -INFINITY, mrun = -INFINITY, l = 0.f;
  unsigned rd = ring, wr = ring;
  u32x4 q0, q1, q2, q3;
  asm volatile("s_waitcnt vmcnt(8)\n\tds_read_b128 %0, %4\n\tds_read_b128 %1, %4 offset:64\n\tds_read_b128 %2, %4 offset:128\n\t"
               "ds_read_b128 %3, %4 offset:192\n\ts_waitcnt lgkmcnt(0)"
               : "=&v"(q0), "=&v"(q1), "=&v"(q2), "=&v"(q3) : "v"(qst + (c & 3) * 256 + g * 16) : "memory");

  asm volatile("" : "+v"(pfd));
#define ADVANCE() do { wr = rd; rd += TILE_BYTES; if (rd == ring + RING_BYTES) rd = ring; } while (0)
#define PROGRESS(h) asm volatile("s_mov_b64 exec, 1\n\tds_max_u32 %0, %1\n\ts_mov_b64 exec, -1" :: "v"(ldsb + TK_OFF + 252), "v"(h) : "memory")
#define K_ADDR() unsigned a0 = rd + ka, a1 = a0 ^ 64, a2 = a0 ^ 128, a3 = a0 ^ 192; asm volatile("" : "+v"(a0), "+v"(a1), "+v"(a2), "+v"(a3))
#define K_STEP(sc) do {                    \
    u32x4 k0, k1, k2, k3;                                                                                           \
    asm volatile("ds_read_b128 %0, %4\n\tds_read_b128 %1, %5\n\tds_read_b128 %2, %6\n\tds_read_b128 %3, %7\n\ts_waitcnt lgkmcnt(0)" \
                 : "=&v"(k0), "=&v"(k1), "=&v"(k2), "=&v"(k3) : "v"(a0), "v"(a1), "v"(a2), "v"(a3) : "memory");      \
    f32x4 s = {0.f, 0.f, 0.f, 0.f};                                                                                 \
    s = __builtin_amdgcn_mfma_f32_16x16x32_bf16(__builtin_bit_cast(bf16x8, k0), __builtin_bit_cast(bf16x8, q0), s, 0, 0, 0); \
    s = __builtin_amdgcn_mfma_f32_16x16x32_bf16(__builtin_bit_cast(bf16x8, k1), __builtin_bit_cast(bf16x8, q1), s, 0, 0, 0); \
    s = __builtin_amdgcn_mfma_f32_16x16x32_bf16(__builtin_bit_cast(bf16x8, k2), __builtin_bit_cast(bf16x8, q2), s, 0, 0, 0); \
    s = __builtin_amdgcn_mfma_f32_16x16x32_bf16(__builtin_bit_cast(bf16x8, k3), __builtin_bit_cast(bf16x8, q3), s, 0, 0, 0); \
    mrun = fmaxf(mrun, fmaxf(fmaxf(s[0], s[1]), fmaxf(s[2], s[3])) * C2);                                           \
    *LDSP(f32x4, sc) = s;                                                                                           \
    ADVANCE();                                                                                                      \
  } while (0)

#pragma unroll 1
  for (int h = 0; h < S_SEP; ++h) {
    K_ADDR();
    if (h != 0) {
      const bool nv = h + 2 >= S_SEP;
      DMA_TILE(nv ? V : K, wr, soff0 + (unsigned)(nv ? h + 2 - S_SEP : h + 2) * ROUND_BYTES);
      WAIT_VM(8);
    }
    K_STEP(sa + 256 * h);
  }
  f32x4 acc0 = {0.f, 0.f, 0.f, 0.f}, acc1 = acc0;
  unsigned reca = ldsb + RED_OFF + (w * 4 + (c & 3)) * RED_REC + (c >> 2) * 64 + g * 16;
  unsigned recw = ldsb + RED_OFF + (w * 4 + (c & 3)) * RED_REC + DH * 4;
  unsigned recb = ldsb + RED_OFF + w * RED_REC + (lane & 31) * 16;
  unsigned recm = ldsb + RED_OFF + w * RED_REC + DH * 4;
  const unsigned mb = (lane & 31) * 16 + w * 4096 + j * 512;
  unsigned mb0 = mb | (lane & 32) << 20, mb1 = mb ^ (lane < 32 ? 512 : 1024), mb3 = mb ^ 1536;
  unsigned oo = lane * 8;
  asm volatile("" : "+v"(reca), "+v"(recw), "+v"(recb), "+v"(recm), "+v"(mb0), "+v"(mb1), "+v"(mb3), "+v"(oo));
  unsigned snap = 0;
  unsigned noff = 0;
  bool more = true, fin = false;
  u32x2 pk0, pk1, pk2, pk3; unsigned b0, b1, b2, b3, b4, b5, b6, b7;
#define PV(acc, va_, vb_, pa_, pb_) acc = __builtin_amdgcn_mfma_f32_16x16x32_bf16(__builtin_bit_cast(bf16x8, __builtin_shufflevector(va_, vb_, 0, 1, 2, 3)), \
                                                                            __builtin_bit_cast(bf16x8, __builtin_shufflevector(pa_, pb_, 0, 1, 2, 3)), acc, 0, 0, 0)
#define V_MMA() do {                                                                                                \
    u32x2 v0, v1, v2, v3, v4, v5, v6, v7;                                                                           \
    asm volatile("ds_read_b64_tr_b16 %0, %8\n\tds_read_b64_tr_b16 %1, %9\n\tds_read_b64_tr_b16 %2, %10\n\tds_read_b64_tr_b16 %3, %11\n\t" \
                 "ds_read_b64_tr_b16 %4, %12\n\tds_read_b64_tr_b16 %5, %13\n\tds_read_b64_tr_b16 %6, %14\n\tds_read_b64_tr_b16 %7, %15\n\t" \
                 "s_waitcnt lgkmcnt(0)"                                                                             \
                 : "=&v"(v0), "=&v"(v1), "=&v"(v2), "=&v"(v3), "=&v"(v4), "=&v"(v5), "=&v"(v6), "=&v"(v7)            \
                 : "v"(b0), "v"(b1), "v"(b2), "v"(b3), "v"(b4), "v"(b5), "v"(b6), "v"(b7) : "memory");               \
    PV(acc0, v0, v1, pk0, pk1); PV(acc1, v4, v5, pk0, pk1); PV(acc0, v2, v3, pk2, pk3); PV(acc1, v6, v7, pk2, pk3); \
  } while (0)
#pragma unroll 1
  for (int u = 0;; ++u) {
    if (u >= S_SEP) {
      K_ADDR();
      PROGRESS(2 * u);
      unsigned t;
      do t = __builtin_amdgcn_readfirstlane(*LDSP(volatile unsigned, ldsb + TK_OFF + 4 * (u - S_SEP))); while (t == NOTREADY);
      more = t < NDYN;
      fin |= t == NDYN - 1;
      noff = woff + (S_IT * GRP + t) * UNIT_BYTES;
      if (more) {
        DMA_TILE(K, wr, noff);
        WAIT_VM(8);
      } else {
        snap = *LDSP(volatile unsigned, ldsb + MSK_OFF + (w * 64 + lane) * 4);
        WAIT_VM(4);
      }
      K_STEP(sa);
    }
    if (u > U_SYNC) PROGRESS(u < S_SEP ? S_SEP + u : 2 * u + 1);
    b0 = rd + va; b1 = b0 ^ 32; b2 = b0 ^ 64; b3 = b0 ^ 96; b4 = b0 ^ 128; b5 = b0 ^ 160; b6 = b0 ^ 192; b7 = b0 ^ 224;
    if (__builtin_amdgcn_ballot_w64(mrun > m) != 0) {
      const float mn = xg_max(mrun);
      const float al = __builtin_amdgcn_exp2f(m - mn);
      m = mn; l *= al; acc0 *= al; acc1 *= al;
    }
    const f32x4 s = *LDSP(const f32x4, sa + 256 * (unsigned)(u < S_SEP ? u : 0));
    const float p0 = __builtin_amdgcn_exp2f(fmaf(s[0], C2, -m)), p1 = __builtin_amdgcn_exp2f(fmaf(s[1], C2, -m));
    const float p2 = __builtin_amdgcn_exp2f(fmaf(s[2], C2, -m)), p3 = __builtin_amdgcn_exp2f(fmaf(s[3], C2, -m));
    l += (p0 + p1) + (p2 + p3);
    const u32x2 pk = {cvtpk(p0, p1), cvtpk(p2, p3)}, pz = {0u, 0u};
    pk0 = (c >> 2) == 0 ? pk : pz; pk1 = (c >> 2) == 1 ? pk : pz; pk2 = (c >> 2) == 2 ? pk : pz; pk3 = (c >> 2) == 3 ? pk : pz;
    asm volatile("" : "+v"(pk0), "+v"(pk1), "+v"(pk2), "+v"(pk3), "+v"(b0), "+v"(b1), "+v"(b2), "+v"(b3), "+v"(b4), "+v"(b5), "+v"(b6), "+v"(b7));
    if (!more) break;
    DMA_TILE(u == S_SEP - 2 ? K : V, wr, u >= S_SEP ? noff : soff0 + (unsigned)min(u + 2, S_SEP) * ROUND_BYTES);
    if (u == U_SYNC) __builtin_amdgcn_s_barrier();
    WAIT_VM(8);
    V_MMA();
    ADVANCE();
  }

  *LDSP(f32x2, recw) = f32x2{m, xg_sum(l)};
  unsigned sb[8];
#pragma unroll
  for (int y = 0; y < 8; ++y) sb[y] = (fin ? snap : ~snap >> 8) >> y;
  asm volatile("" : "+v"(sb[0]), "+v"(sb[1]), "+v"(sb[2]), "+v"(sb[3]), "+v"(sb[4]), "+v"(sb[5]), "+v"(sb[6]), "+v"(sb[7]));
  u32x4 x[NPOLL][2];
#define POLL(k) asm volatile(LOAD2("%0, %2, %4", "%1, %3, %4") : "=&v"(x[k][0]), "=&v"(x[k][1]) : "v"(mb1), "v"(mb3), "s"(gws) : "memory")
  WAIT_VM(0);
  V_MMA();
  *LDSP(f32x4, reca) = acc0;
  *LDSP(f32x4, reca + 256) = acc1;
  if (fin) POLL(0);
  f32x2 ml[NW]; f32x4 o[NW];
  asm volatile("s_waitcnt lgkmcnt(0)\n\ts_barrier\n\t"
               "ds_read_b64 %0, %8\n\tds_read_b64 %1, %8 offset:%10\n\tds_read_b64 %2, %8 offset:%11\n\tds_read_b64 %3, %8 offset:%12\n\t"
               "ds_read_b128 %4, %9\n\tds_read_b128 %5, %9 offset:%10\n\tds_read_b128 %6, %9 offset:%11\n\tds_read_b128 %7, %9 offset:%12\n\t"
               "s_waitcnt lgkmcnt(0)"
               : "=&v"(ml[0]), "=&v"(ml[1]), "=&v"(ml[2]), "=&v"(ml[3]), "=&v"(o[0]), "=&v"(o[1]), "=&v"(o[2]), "=&v"(o[3])
               : "v"(recm), "v"(recb), "i"(4 * RED_REC), "i"(8 * RED_REC), "i"(12 * RED_REC) : "memory");
  const float M0 = fmaxf(fmaxf(ml[0][0], ml[1][0]), fmaxf(ml[2][0], ml[3][0]));
  const float w0 = __builtin_amdgcn_exp2f(ml[0][0] - M0), w1 = __builtin_amdgcn_exp2f(ml[1][0] - M0), w2 = __builtin_amdgcn_exp2f(ml[2][0] - M0), w3 = __builtin_amdgcn_exp2f(ml[3][0] - M0);
  const f32x4 r = (o[0] * w0 + o[1] * w1) + (o[2] * w2 + o[3] * w3);
  float L0 = fmaf(w0, ml[0][1], w1 * ml[1][1]) + fmaf(w2, ml[2][1], w3 * ml[3][1]);
  if (!fin) {
    const u32x4 v = __builtin_bit_cast(u32x4, r);
    const unsigned mbit = __float_as_uint(M0) >> (lane & 31), lbit = __float_as_uint(L0) >> (lane & 31);
    const u32x4 u = {(v[0] & ~3u) | ((mbit & 1) << 1) | (sb[0] & 1), (v[1] & ~3u) | ((lbit & 1) << 1) | (sb[1] & 1), (v[2] & ~1u) | (sb[2] & 1), (v[3] & ~1u) | (sb[3] & 1)};
    __builtin_amdgcn_raw_buffer_store_b128(u, __builtin_amdgcn_make_buffer_rsrc((void*)gws, 0, WS_CTL, 0x00020000), mb0, 0, 16);
  } else {
    POLL(1);
    u32x4 xa, xb;
    asm("" : "=v"(xa), "=v"(xb));
    bool done = false;
#pragma unroll 1
    for (unsigned n = 0; n < POLL_CAP && !done; ++n) {
#pragma unroll
      for (int k = 0; k < NPOLL; ++k) {
        asm volatile("s_waitcnt vmcnt(%2)" : "+v"(x[k][0]), "+v"(x[k][1]) : "i"(2 * (NPOLL - 1)) : "memory");
        unsigned fresh = 1;
#pragma unroll
        for (int y = 0; y < 8; ++y) fresh &= x[k][y >> 2][y & 3] ^ sb[y];
        if (__builtin_amdgcn_ballot_w64(fresh == 0) == 0) { xa = x[k][0]; xb = x[k][1]; done = true; break; }
        POLL(k);
      }
    }
    if (!done) L0 = __uint_as_float(~0u);
    const unsigned long long ma = __builtin_amdgcn_ballot_w64((xa[0] & 2) != 0), la = __builtin_amdgcn_ballot_w64((xa[1] & 2) != 0);
    const float M1 = __uint_as_float((unsigned)ma), M2 = __uint_as_float((unsigned)(ma >> 32)), M3 = __uint_as_float((unsigned)__builtin_amdgcn_ballot_w64((xb[0] & 2) != 0));
    const float L1 = __uint_as_float((unsigned)la), L2 = __uint_as_float((unsigned)(la >> 32)), L3 = __uint_as_float((unsigned)__builtin_amdgcn_ballot_w64((xb[1] & 2) != 0));
    const float M = fmaxf(fmaxf(M0, M1), fmaxf(M2, M3));
    const float ca = __builtin_amdgcn_exp2f((lane < 32 ? M1 : M2) - M), cb = __builtin_amdgcn_exp2f((lane < 32 ? M3 : M0) - M);
    const u32x4 keep = {~3u, ~3u, ~1u, ~1u};
    const f32x4 vb = lane < 32 ? __builtin_bit_cast(f32x4, xb & keep) : r;
    const f32x4 y = __builtin_bit_cast(f32x4, xa & keep) * ca + vb * cb;
    const float ls = fmaf(ca, lane < 32 ? L1 : L2, cb * (lane < 32 ? L3 : L0));
    auto half_sum = [](float v) { auto t = __builtin_amdgcn_permlane32_swap(__float_as_uint(v), __float_as_uint(v), false, false); return __uint_as_float(t[0]) + __uint_as_float(t[1]); };
    const float inv = __builtin_amdgcn_rcpf(half_sum(ls));
    const u32x2 res = {cvtpk(half_sum(y[0]) * inv, half_sum(y[1]) * inv), cvtpk(half_sum(y[2]) * inv, half_sum(y[3]) * inv)};
    __builtin_amdgcn_raw_buffer_store_b64(res, __builtin_amdgcn_make_buffer_rsrc((void*)(out + (pair * 4 + w) * 64), 0, 256, 0x00020000), oo, 0, 0);
#pragma unroll
    for (int k = 0; k < NPOLL; ++k) asm volatile("s_waitcnt vmcnt(0)" : "+v"(x[k][0]), "+v"(x[k][1]) :: "memory");
  }
}

extern "C" void kernel_launch(void* const* d_in, const int* in_sizes, int n_in, void* d_out, int out_size, void* d_ws, size_t ws_size, hipStream_t stream) {
  (void)hipFuncSetAttribute((const void*)gqa_decode, hipFuncAttributeMaxDynamicSharedMemorySize, LDS_BYTES);
  gqa_decode<<<NPAIR * GRP, (NW + 1) * 64, LDS_BYTES, stream>>>((const char*)d_in[1], (const char*)d_in[2], (const char*)d_in[0], (char*)d_ws, (unsigned*)d_out);
}
```

```cpp
#include <hip/hip_runtime.h>
#include <hip/hip_bf16.h>

typedef unsigned u32x8 __attribute__((ext_vector_type(8)));
typedef unsigned u32x4 __attribute__((ext_vector_type(4)));
typedef unsigned u32x2 __attribute__((ext_vector_type(2)));
typedef float f32x4 __attribute__((ext_vector_type(4)));
typedef __bf16 bf16x8 __attribute__((ext_vector_type(8)));
typedef __bf16 bf16x2 __attribute__((ext_vector_type(2)));
#define LDSP(T, a) ((__attribute__((address_space(3))) T*)(size_t)(a))
typedef __attribute__((address_space(1))) unsigned gu32;
#define RLX_AGENT __ATOMIC_RELAXED, __HIP_MEMORY_SCOPE_AGENT

constexpr int SK = 8192, DH = 128, NPAIR = 64, GRP = 4, NW = 4;
constexpr int TILE = 16;
constexpr int NSTEP = SK / (TILE * NW * GRP);
constexpr unsigned PAIR_BYTES = SK * DH * 2;
constexpr unsigned TILE_BYTES = TILE * DH * 2;
constexpr int T_OFF = -60, T_STEP = 852, T_SLEW = 300;
constexpr int PHASE_A = 0, PHASE_B = 300, PHASE_C = 580;
constexpr unsigned RING_BYTES = 3 * TILE_BYTES;
constexpr unsigned SC_OFF = NW * RING_BYTES, SC_BYTES = NSTEP * 256 + 1024;
constexpr unsigned RED_OFF = SC_OFF + NW * SC_BYTES;
constexpr unsigned RED_REC = (DH + 4) * 4;
constexpr unsigned MSK_OFF = RED_OFF + NW * 4 * RED_REC;
constexpr unsigned DIG_OFF = MSK_OFF + 1024;
constexpr unsigned BASE_OFF = DIG_OFF + 256;
constexpr unsigned LDS_BYTES = BASE_OFF + 16;
static_assert(LDS_BYTES > 80 * 1024 && LDS_BYTES <= 160 * 1024, "exactly one workgroup per CU");
constexpr unsigned G_WS = 32768, WS_CTL = 16384, WS_DIG = WS_CTL + 512;
constexpr unsigned BEAT2 = 1u << 24;
constexpr unsigned BEAT_CAP = 1u << 20, POLL_CAP = 1u << 21;
constexpr float C2 = 0.088388347648318440f * 1.4426950408889634f;

__device__ __forceinline__ unsigned cvtpk(float lo, float hi) {
  bf16x2 v = {(__bf16)lo, (__bf16)hi};
  return __builtin_bit_cast(unsigned, v);
}
__device__ __forceinline__ float xg_max(float x) {
  auto s = __builtin_amdgcn_permlane16_swap(__float_as_uint(x), __float_as_uint(x), false, false);
  x = fmaxf(__uint_as_float(s[0]), __uint_as_float(s[1]));
  auto t = __builtin_amdgcn_permlane32_swap(__float_as_uint(x), __float_as_uint(x), false, false);
  return fmaxf(__uint_as_float(t[0]), __uint_as_float(t[1]));
}
__device__ __forceinline__ float xg_sum(float x) {
  auto s = __builtin_amdgcn_permlane16_swap(__float_as_uint(x), __float_as_uint(x), false, false);
  x = __uint_as_float(s[0]) + __uint_as_float(s[1]);
  auto t = __builtin_amdgcn_permlane32_swap(__float_as_uint(x), __float_as_uint(x), false, false);
  return __uint_as_float(t[0]) + __uint_as_float(t[1]);
}
__device__ __forceinline__ unsigned lsb4(u32x4 x) { return (x[0] & 1) | ((x[1] & 1) << 1) | ((x[2] & 1) << 2) | ((x[3] & 1) << 3); }
__device__ __forceinline__ int sum8(int x) {
  int s = 0;
#pragma unroll
  for (int y = 0; y < 8; ++y) s += __builtin_amdgcn_readlane(x, y);
  return s;
}
__device__ __forceinline__ unsigned clock16() {
  unsigned long long t;
  asm volatile("s_memrealtime %0\n\ts_waitcnt lgkmcnt(0)" : "=s"(t) :: "memory");
  return (unsigned)t * 16;
}

#define DMA_TILE(p, m0v)                                                                                            \
  asm volatile("s_mov_b32 m0, %0\n\ts_nop 0\n\t"                                                                    \
               "buffer_load_dwordx4 %3, %1, %2 offen nt lds\n\tbuffer_load_dwordx4 %4, %1, %2 offen offset:1024 nt lds\n\t" \
               "buffer_load_dwordx4 %5, %1, %2 offen offset:2048 nt lds\n\tbuffer_load_dwordx4 %6, %1, %2 offen offset:3072 nt lds" \
               :: "s"(m0v), "s"(__builtin_amdgcn_make_buffer_rsrc((void*)(p), 0, NPAIR * PAIR_BYTES, 0x00020000)), "s"(poff), \
                  "v"(so0), "v"(so1), "v"(so2), "v"(so3) : "memory")
#define WAIT_VM(n) asm volatile("s_waitcnt vmcnt(" #n ")" ::: "memory")
#define LOAD2(x1, x2) "global_load_dwordx4 " x1 " sc1\n\tglobal_load_dwordx4 " x2 " sc1\n\t"

__global__ void __launch_bounds__(320, 2) gqa_decode(const char* K_, const char* V_, const char* Q_, char* ws_, unsigned* out_) {
  extern __shared__ __attribute__((aligned(256))) char lds[];
  u32x8 arg8; u32x2 arg2;
  asm volatile("s_load_dwordx8 s[40:47], %2, 0x0\n\ts_load_dwordx2 s[48:49], %2, 0x20"
               : "=&{s[40:47]}"(arg8), "=&{s[48:49]}"(arg2) : "s"(__builtin_amdgcn_kernarg_segment_ptr()));
  unsigned pfd;
  {
    unsigned long long pc;
    asm volatile("s_getpc_b64 %0" : "=s"(pc));
    asm volatile("global_load_dword %0, %1, %2" : "=&{v120}"(pfd) : "v"(min(threadIdx.x * 16, 4096u)), "s"(pc) : "memory");
  }
  __builtin_amdgcn_sched_barrier(0);
  const unsigned bid = blockIdx.x, j = (bid & 1) | ((bid >> 1) & 2), pair = (bid >> 3) * 2 + (((bid >> 1) ^ (bid & (bid >> 5))) & 1);
  const unsigned lane = threadIdx.x & 63, w = __builtin_amdgcn_readfirstlane(threadIdx.x >> 6);
  const unsigned g = lane >> 4, c = lane & 15;
  const unsigned sox = (((g & 1) * 0x811 ^ (g >> 1) * 0x222 ^ (w & 1) * 0x201 ^ (w >> 1) * 0x102) << 8) | ((c ^ (g << 2)) << 4);
  unsigned so0, so1, so2, so3;
#define SET_UNIT(n) do { const unsigned n_ = (n); so0 = sox ^ ((n_ << 14) ^ ((n_ & 1) << 16) ^ ((n_ & 4) << 17)); so1 = so0 ^ 32; so2 = so0 ^ 48; so3 = so0 ^ 16; } while (0)
  SET_UNIT(j);
  const unsigned ldsb = (unsigned)(size_t)lds;
  const unsigned ring = ldsb + w * RING_BYTES;
  const unsigned poff = pair * PAIR_BYTES;
  asm volatile("s_mov_b32 m0, %2\n\ts_cmp_eq_u32 %8, %9\n\ts_waitcnt lgkmcnt(0)\n\ts_cbranch_scc1 1f\n\t"
               "s_mov_b32 s52, s40\n\ts_and_b32 s53, s41, 0xffff\n\t"
               "buffer_load_dwordx4 %4, s[52:55], %3 offen nt lds\n\tbuffer_load_dwordx4 %5, s[52:55], %3 offen offset:1024 nt lds\n\t"
               "buffer_load_dwordx4 %6, s[52:55], %3 offen offset:2048 nt lds\n\tbuffer_load_dwordx4 %7, s[52:55], %3 offen offset:3072 nt lds\n1:"
               : "+{s[40:47]}"(arg8), "+{s[48:49]}"(arg2)
               : "s"(ring), "s"(poff), "v"(so0), "v"(so1), "v"(so2), "v"(so3), "s"(w), "i"(NW), "{s54}"(NPAIR * PAIR_BYTES), "{s55}"(0x00020000)
               : "memory", "scc", "s52", "s53");
  auto ptr = [](unsigned lo, unsigned hi) { return (char*)(__attribute__((address_space(1))) char*)(((unsigned long long)hi << 32) | lo); };
  const char* const K = ptr(arg8[0], arg8[1]);
  const char* const V = ptr(arg8[2], arg8[3]);
  const char* const Q = ptr(arg8[4], arg8[5]);
  char* const ws = ptr(arg8[6], arg8[7]);
  unsigned* const out = (unsigned*)ptr(arg2[0], arg2[1]);
  char* const gws = ws + (size_t)pair * G_WS;
  const unsigned t0 = clock16();

  if (w == NW) {
    __builtin_amdgcn_s_setprio(3);
    asm volatile("s_waitcnt vmcnt(0)" :: "{v120}"(pfd) : "memory");
    gu32* const ctl = (gu32*)(ws + (size_t)(pair | 1) * G_WS + WS_CTL);
    const unsigned me = (pair & 1) * 4 + j;
    unsigned e0; u32x4 a0, a1;
    asm volatile("global_load_dword %0, %3, %5 sc1\n\t" LOAD2("%1, %4, %5", "%2, %6, %5") "s_waitcnt vmcnt(0)"
                 : "=&v"(e0), "=&v"(a0), "=&v"(a1)
                 : "v"((~pair & 1) * G_WS + WS_CTL + (lane & 7) * 4), "v"((lane & 31) * 16 + j * 512 + (lane >> 5) * 4096), "s"(gws),
                   "v"((lane & 31) * 16 + j * 512 + (lane >> 5) * 4096 + 8192) : "memory");
    if (lane == 0) __hip_atomic_fetch_add(ctl + me, (t0 & 0xFFFF0u) | 1u, RLX_AGENT);
    *LDSP(volatile unsigned char, ldsb + DIG_OFF + lane) = (unsigned char)(lsb4(a0) | (lsb4(a1) << 4));
    u32x4 dg = *LDSP(volatile u32x4, ldsb + DIG_OFF + (lane & 3) * 16);
    if (lane >= 4) dg = u32x4{t0, t0, t0, t0};
    __builtin_amdgcn_raw_buffer_store_b128(dg, __builtin_amdgcn_make_buffer_rsrc((void*)(gws + WS_DIG + j * 128), 0, 128, 0x00020000), lane * 16, 0, 16);
    const bool other = lane < 8 && lane != me;
    bool fail = true, second = false;
#pragma unroll 1
    for (unsigned n = 0; n < BEAT_CAP; ++n) {
      const unsigned d = __hip_atomic_load(ctl + (lane & 7), RLX_AGENT) - e0;
      if (!second && __builtin_amdgcn_ballot_w64(other && d == 0) == 0) {
        second = true;
        *LDSP(volatile unsigned, ldsb + BASE_OFF + (lane & 3) * 4) = t0 + (sum8(other && (d & 1) ? (int)((d - 1 - t0) << 12) >> 12 : 0) >> 3);
        WAIT_VM(0);
        if (lane == 0) __hip_atomic_fetch_add(ctl + me, BEAT2, RLX_AGENT);
      }
      if (second && __builtin_amdgcn_ballot_w64(other && d < BEAT2) == 0) { fail = false; break; }
      __builtin_amdgcn_s_sleep(8);
    }
    unsigned dd, dt;
    asm volatile("global_load_dword %0, %2, %4 sc1\n\tglobal_load_dword %1, %3, %5 sc1\n\ts_waitcnt vmcnt(0)"
                 : "=&v"(dd), "=&v"(dt)
                 : "v"(WS_DIG + (j ^ (lane >> 4)) * 128 + (lane & 15) * 4), "v"(((lane >> 2) & 1) * G_WS + WS_DIG + (lane & 3) * 128 + 64), "s"(gws),
                   "s"(ws + (size_t)(pair & ~1u) * G_WS) : "memory");
    *LDSP(volatile unsigned, ldsb + BASE_OFF + (lane & 3) * 4) = t0 + (sum8((int)(dt - t0)) >> 3);
    *LDSP(volatile unsigned, ldsb + DIG_OFF + lane * 4) = dd;
#pragma unroll
    for (int y = 0; y < NW; ++y) {
      const unsigned db = ldsb + DIG_OFF + (y & 1) * 32 + (lane & 31);
      const unsigned n0 = *LDSP(volatile unsigned char, db), n1 = *LDSP(volatile unsigned char, db + 64 + (lane & 32) * 2), n3 = *LDSP(volatile unsigned char, db + 192);
      *LDSP(volatile unsigned, ldsb + MSK_OFF + y * 256 + lane * 4) =
          ((n1 >> 4 * (y >> 1)) & 15) | (((n3 >> 4 * (y >> 1)) & 15) << 4) | (((n0 >> 4 * (y >> 1)) & 15) << 8) | (fail ? 1u << 31 : 0);
    }
    asm volatile("s_waitcnt lgkmcnt(0)\n\ts_barrier\n\ts_barrier" ::: "memory");
    return;
  }

  const unsigned qst = ldsb + SC_OFF + (w + 1) * SC_BYTES - 1024;
  asm volatile("s_mov_b32 m0, %0\n\ts_nop 0\n\tglobal_load_lds_dwordx4 %1, %2" :: "s"(qst), "v"(lane * 16), "s"(Q + (size_t)pair * 1024) : "memory");
  const unsigned xcc = __builtin_amdgcn_s_getreg(20 | (0 << 6) | (3 << 11));
  const int phase = ((xcc + 2) & 4) == 0 ? PHASE_A : (xcc & 1) ? PHASE_C : PHASE_B;
  unsigned due = t0 + T_OFF;
  *LDSP(volatile unsigned, ldsb + BASE_OFF + w * 4) = t0;
#define BASE_ADJ() (int)(__builtin_amdgcn_readfirstlane(*LDSP(volatile unsigned, ldsb + BASE_OFF + w * 4)) - t0 + phase)
#define PACE(adj) do { due += T_STEP; const unsigned d_ = due + (adj); asm volatile("" : "+v"(so0), "+v"(so1), "+v"(so2), "+v"(so3)); \
    while (d_ - clock16() - 1 < 16384u) __builtin_amdgcn_s_sleep(1);     } while (0)
#pragma unroll 1
  for (int n = 1; n <= 2; ++n) {
    SET_UNIT(n * GRP + j);
    PACE(0);
    DMA_TILE(K, ring + n * TILE_BYTES);
  }

  const unsigned fc = ((c & 3) << 2) | ((((c >> 2) ^ (c >> 3)) & 1) << 1) | (c >> 3);
  const unsigned ka = c * 256 + ((g ^ fc) << 4);
  const unsigned qq = (lane >> 2) & 3, pp = lane & 3;
  const unsigned fr = (qq << 2) | (((g ^ (g >> 1)) & 1) << 1) | (g >> 1);
  unsigned va = (4 * g + qq) * 256 + (((pp >> 1) ^ fr) << 4) + 8 * (pp & 1);
  const unsigned sa = ldsb + SC_OFF + w * SC_BYTES + (c & 3) * 64 + g * 16;
  unsigned reca = ldsb + RED_OFF + (w * 4 + (c & 3)) * RED_REC + (c >> 2) * 64 + g * 16;
  unsigned recw = ldsb + RED_OFF + (w * 4 + (c & 3)) * RED_REC + DH * 4;
  unsigned recc = ldsb + RED_OFF + (c & 3) * RED_REC + DH * 4;
  unsigned recb = ldsb + RED_OFF + w * RED_REC + (lane & 31) * 16;
  unsigned recl = ldsb + RED_OFF + w * RED_REC + DH * 4 + 4;
  unsigned mska = ldsb + MSK_OFF + (w * 64 + lane) * 4;
  const unsigned mb = (lane & 31) * 16 + w * 4096 + j * 512;
  unsigned mb0 = mb | (lane & 32) << 20, mb1 = mb ^ (lane < 32 ? 512 : 1024), mb3 = mb ^ 1536;
  unsigned oo = (lane & 31) * 8 + (lane >> 5) * 4;
  asm volatile("" : "+v"(reca), "+v"(recw), "+v"(recc), "+v"(recb), "+v"(recl), "+v"(mska), "+v"(mb0), "+v"(mb1), "+v"(mb3), "+v"(oo), "+v"(va));
  u32x4 q0, q1, q2, q3;
  asm volatile("s_waitcnt vmcnt(8)\n\tds_read_b128 %0, %4\n\tds_read_b128 %1, %4 offset:64\n\tds_read_b128 %2, %4 offset:128\n\t"
               "ds_read_b128 %3, %4 offset:192\n\ts_waitcnt lgkmcnt(0)"
               : "=&v"(q0), "=&v"(q1), "=&v"(q2), "=&v"(q3) : "v"(qst + (c & 3) * 256 + g * 16), "{v120}"(pfd) : "memory");
  unsigned rd = ring, wr = ring;
#define ADVANCE() do { wr = rd; rd += TILE_BYTES; if (rd == ring + RING_BYTES) rd = ring; } while (0)

  float m = -INFINITY;
  int adj = 0;
#pragma unroll 1
  for (int h = 0; h < NSTEP; ++h) {
    unsigned a0 = rd + ka, a1 = a0 ^ 64, a2 = a0 ^ 128, a3 = a0 ^ 192;
    asm volatile("" : "+v"(a0), "+v"(a1), "+v"(a2), "+v"(a3));
    if (h != 0) {
      SET_UNIT(((h + 2) & (NSTEP - 1)) * GRP + j);
      adj += min(max(BASE_ADJ() - adj, -T_SLEW), T_SLEW);
      PACE(adj);
      DMA_TILE(h + 2 >= NSTEP ? V : K, wr);
      WAIT_VM(8);
    }
    u32x4 k0, k1, k2, k3;
    asm volatile("ds_read_b128 %0, %4\n\tds_read_b128 %1, %5\n\tds_read_b128 %2, %6\n\tds_read_b128 %3, %7\n\ts_waitcnt lgkmcnt(0)"
                 : "=&v"(k0), "=&v"(k1), "=&v"(k2), "=&v"(k3) : "v"(a0), "v"(a1), "v"(a2), "v"(a3) : "memory");
    f32x4 s = {0.f, 0.f, 0.f, 0.f};
    s = __builtin_amdgcn_mfma_f32_16x16x32_bf16(__builtin_bit_cast(bf16x8, k0), __builtin_bit_cast(bf16x8, q0), s, 0, 0, 0);
    s = __builtin_amdgcn_mfma_f32_16x16x32_bf16(__builtin_bit_cast(bf16x8, k1), __builtin_bit_cast(bf16x8, q1), s, 0, 0, 0);
    s = __builtin_amdgcn_mfma_f32_16x16x32_bf16(__builtin_bit_cast(bf16x8, k2), __builtin_bit_cast(bf16x8, q2), s, 0, 0, 0);
    s = __builtin_amdgcn_mfma_f32_16x16x32_bf16(__builtin_bit_cast(bf16x8, k3), __builtin_bit_cast(bf16x8, q3), s, 0, 0, 0);
    m = fmaxf(m, fmaxf(fmaxf(s[0], s[1]), fmaxf(s[2], s[3])) * C2);
    *LDSP(f32x4, sa + 256 * h) = s;
    ADVANCE();
  }
  due += adj;

  *LDSP(volatile float, recw) = xg_max(m);
  asm volatile("s_waitcnt lgkmcnt(0)\n\ts_barrier" ::: "memory");
  m = fmaxf(fmaxf(*LDSP(volatile float, recc), *LDSP(volatile float, recc + 4 * RED_REC)),
            fmaxf(*LDSP(volatile float, recc + 8 * RED_REC), *LDSP(volatile float, recc + 12 * RED_REC)));
  const float M0 = __uint_as_float(__builtin_amdgcn_readlane(__float_as_uint(m), w));
  const bool fin = w == j;
  const unsigned snap = *LDSP(volatile unsigned, mska);
  unsigned sb[8];
#pragma unroll
  for (int y = 0; y < 8; ++y) sb[y] = ((fin ? snap : ~snap >> 8) >> y) & 1;
  sb[0] |= __builtin_amdgcn_ubfe(__float_as_uint(M0), lane & 31, 1) << 1;
  const __amdgpu_buffer_rsrc_t rdst = __builtin_amdgcn_make_buffer_rsrc(fin ? (void*)(out + (pair * 4 + w) * 64) : (void*)gws, 0, fin ? 256 : WS_CTL, 0x00020000);
  asm volatile("" : "+v"(sb[0]), "+v"(sb[1]), "+v"(sb[2]), "+v"(sb[3]), "+v"(sb[4]), "+v"(sb[5]), "+v"(sb[6]), "+v"(sb[7]) : "s"(rdst));
  float l = (int)snap < 0 ? __uint_as_float(~0u) : 0.f;

  f32x4 acc0 = {0.f, 0.f, 0.f, 0.f}, acc1 = acc0;
#define V_PREP(u)                                       \
    unsigned b0 = rd + va, b1 = b0 ^ 32, b2 = b0 ^ 64, b3 = b0 ^ 96, b4 = b0 ^ 128, b5 = b0 ^ 160, b6 = b0 ^ 192, b7 = b0 ^ 224; \
    const f32x4 s = *LDSP(const f32x4, sa + 256 * (u));                                                             \
    const float p0 = __builtin_amdgcn_exp2f(fmaf(s[0], C2, -m)), p1 = __builtin_amdgcn_exp2f(fmaf(s[1], C2, -m));   \
    const float p2 = __builtin_amdgcn_exp2f(fmaf(s[2], C2, -m)), p3 = __builtin_amdgcn_exp2f(fmaf(s[3], C2, -m));   \
    l += (p0 + p1) + (p2 + p3);                                                                                     \
    const u32x2 pk = {cvtpk(p0, p1), cvtpk(p2, p3)}, pz = {0u, 0u};                                                 \
    u32x2 pk0 = (c >> 2) == 0 ? pk : pz, pk1 = (c >> 2) == 1 ? pk : pz, pk2 = (c >> 2) == 2 ? pk : pz, pk3 = (c >> 2) == 3 ? pk : pz; \
    asm volatile("" : "+v"(pk0), "+v"(pk1), "+v"(pk2), "+v"(pk3), "+v"(b0), "+v"(b1), "+v"(b2), "+v"(b3), "+v"(b4), "+v"(b5), "+v"(b6), "+v"(b7))
#define PV(acc, va_, vb_, pa_, pb_) acc = __builtin_amdgcn_mfma_f32_16x16x32_bf16(__builtin_bit_cast(bf16x8, __builtin_shufflevector(va_, vb_, 0, 1, 2, 3)), \
                                                                            __builtin_bit_cast(bf16x8, __builtin_shufflevector(pa_, pb_, 0, 1, 2, 3)), acc, 0, 0, 0)
#define V_MMA() do {                                                                                                \
    u32x2 v0, v1, v2, v3, v4, v5, v6, v7;                                                                           \
    asm volatile("ds_read_b64_tr_b16 %0, %8\n\tds_read_b64_tr_b16 %1, %9\n\tds_read_b64_tr_b16 %2, %10\n\tds_read_b64_tr_b16 %3, %11\n\t" \
                 "ds_read_b64_tr_b16 %4, %12\n\tds_read_b64_tr_b16 %5, %13\n\tds_read_b64_tr_b16 %6, %14\n\tds_read_b64_tr_b16 %7, %15\n\t" \
                 "s_waitcnt lgkmcnt(0)"                                                                             \
                 : "=&v"(v0), "=&v"(v1), "=&v"(v2), "=&v"(v3), "=&v"(v4), "=&v"(v5), "=&v"(v6), "=&v"(v7)            \
                 : "v"(b0), "v"(b1), "v"(b2), "v"(b3), "v"(b4), "v"(b5), "v"(b6), "v"(b7) : "memory");               \
    PV(acc0, v0, v1, pk0, pk1); PV(acc1, v4, v5, pk0, pk1); PV(acc0, v2, v3, pk2, pk3); PV(acc1, v6, v7, pk2, pk3); \
  } while (0)
#pragma unroll 1
  for (int u = 0; u < NSTEP - 1; ++u) {
    V_PREP(u);
    if (u + 2 < NSTEP) {
      SET_UNIT((u + 2) * GRP + j);
      PACE(0);
      DMA_TILE(V, wr);
      WAIT_VM(8);
    } else WAIT_VM(4);
    V_MMA();
    ADVANCE();
  }
  {
    V_PREP(NSTEP - 1);
    *LDSP(volatile float, recw + 4) = xg_sum(l);
    WAIT_VM(0);
    V_MMA();
  }

  *LDSP(f32x4, reca) = acc0;
  *LDSP(f32x4, reca + 256) = acc1;
  u32x4 xa0, xb0, xa1, xb1;
#define POLL0 LOAD2("v[96:99], %[m1], %[ws]", "v[100:103], %[m3], %[ws]")
#define POLL1 LOAD2("v[104:107], %[m1], %[ws]", "v[108:111], %[m3], %[ws]")
#define POLL_IN [m1] "v"(mb1), [m3] "v"(mb3), [ws] "s"(gws)
  asm volatile("s_cmp_lg_u32 %[t], %[f]\n\ts_cbranch_scc1 1f\n\t" POLL0 "1:"
               : "=&{v[96:99]}"(xa0), "=&{v[100:103]}"(xb0) : POLL_IN, [t] "s"(w), [f] "s"(j) : "memory", "scc");
  float ls[NW]; f32x4 o[NW];
  asm volatile("s_waitcnt lgkmcnt(0)\n\ts_barrier\n\t"
               "ds_read_b32 %0, %8\n\tds_read_b32 %1, %8 offset:%10\n\tds_read_b32 %2, %8 offset:%11\n\tds_read_b32 %3, %8 offset:%12\n\t"
               "ds_read_b128 %4, %9\n\tds_read_b128 %5, %9 offset:%10\n\tds_read_b128 %6, %9 offset:%11\n\tds_read_b128 %7, %9 offset:%12\n\t"
               "s_waitcnt lgkmcnt(0)"
               : "=&v"(ls[0]), "=&v"(ls[1]), "=&v"(ls[2]), "=&v"(ls[3]), "=&v"(o[0]), "=&v"(o[1]), "=&v"(o[2]), "=&v"(o[3])
               : "v"(recl), "v"(recb), "i"(4 * RED_REC), "i"(8 * RED_REC), "i"(12 * RED_REC) : "memory");
  const f32x4 r = (o[0] + o[1]) + (o[2] + o[3]);
  float L0 = (ls[0] + ls[1]) + (ls[2] + ls[3]);
  if (!fin) {
    const u32x4 v = __builtin_bit_cast(u32x4, r);
    const unsigned lo1 = (__builtin_amdgcn_ubfe(__float_as_uint(L0), lane & 31, 1) << 1) | sb[1];
    const u32x4 u = {(v[0] & ~3u) | sb[0], (v[1] & ~3u) | lo1, (v[2] & ~1u) | sb[2], (v[3] & ~1u) | sb[3]};
    __builtin_amdgcn_raw_buffer_store_b128(u, rdst, mb0, 0, 16);
  } else {
    u32x4 xa, xb; unsigned tmp, left = POLL_CAP;
#define FRESH(r0, r1, r2, r3, r4, r5, r6, r7) "s_waitcnt vmcnt(2)\n\t"                                                                    \
    "v_bitop3_b32 %[t], " r0 ", %[s0], 1 bitop3:0x28\n\tv_bitop3_b32 %[t], " r1 ", %[s1], %[t] bitop3:0x28\n\t"                             \
    "v_bitop3_b32 %[t], " r2 ", %[s2], %[t] bitop3:0x28\n\tv_bitop3_b32 %[t], " r3 ", %[s3], %[t] bitop3:0x28\n\t"                          \
    "v_bitop3_b32 %[t], " r4 ", %[s4], %[t] bitop3:0x28\n\tv_bitop3_b32 %[t], " r5 ", %[s5], %[t] bitop3:0x28\n\t"                          \
    "v_bitop3_b32 %[t], " r6 ", %[s6], %[t] bitop3:0x28\n\tv_bitop3_b32 %[t], " r7 ", %[s7], %[t] bitop3:0x28\n\t"                          \
    "v_cmp_eq_u32 vcc, 0, %[t]\n\t"
    asm volatile(POLL1
                 "1:\n\t" FRESH("v96", "v97", "v98", "v99", "v100", "v101", "v102", "v103") "s_cbranch_vccz 2f\n\t" POLL0
                 FRESH("v104", "v105", "v106", "v107", "v108", "v109", "v110", "v111") "s_cbranch_vccz 3f\n\t" POLL1
                 "s_sub_u32 %[n], %[n], 1\n\ts_cbranch_scc0 1b\n\t"
                 "v_mov_b32 %[l], -1\n\ts_branch 4f\n"
                 "3:\n\tv_mov_b64 v[112:113], v[104:105]\n\tv_mov_b64 v[114:115], v[106:107]\n\tv_mov_b64 v[116:117], v[108:109]\n\tv_mov_b64 v[118:119], v[110:111]\n\ts_branch 4f\n"
                 "2:\n\tv_mov_b64 v[112:113], v[96:97]\n\tv_mov_b64 v[114:115], v[98:99]\n\tv_mov_b64 v[116:117], v[100:101]\n\tv_mov_b64 v[118:119], v[102:103]\n"
                 "4:"
                 : "+{v[96:99]}"(xa0), "+{v[100:103]}"(xb0), "=&{v[104:107]}"(xa1), "=&{v[108:111]}"(xb1), "=&{v[112:115]}"(xa), "=&{v[116:119]}"(xb),
                   [t] "=&v"(tmp), [n] "+s"(left), [l] "+v"(L0)
                 : POLL_IN, [s0] "v"(sb[0]), [s1] "v"(sb[1]), [s2] "v"(sb[2]), [s3] "v"(sb[3]), [s4] "v"(sb[4]), [s5] "v"(sb[5]), [s6] "v"(sb[6]), [s7] "v"(sb[7])
                 : "memory", "scc", "vcc");
    const unsigned long long ma = __builtin_amdgcn_ballot_w64((xa[0] & 2) != 0), la = __builtin_amdgcn_ballot_w64((xa[1] & 2) != 0);
    const float M1 = __uint_as_float((unsigned)ma), M2 = __uint_as_float((unsigned)(ma >> 32)), M3 = __uint_as_float((unsigned)__builtin_amdgcn_ballot_w64((xb[0] & 2) != 0));
    const float L1 = __uint_as_float((unsigned)la), L2 = __uint_as_float((unsigned)(la >> 32)), L3 = __uint_as_float((unsigned)__builtin_amdgcn_ballot_w64((xb[1] & 2) != 0));
    const float M = fmaxf(fmaxf(M0, M1), fmaxf(M2, M3));
    const float c0 = __builtin_amdgcn_exp2f(M0 - M), c1 = __builtin_amdgcn_exp2f(M1 - M), c2 = __builtin_amdgcn_exp2f(M2 - M), c3 = __builtin_amdgcn_exp2f(M3 - M);
    const float inv = __builtin_amdgcn_rcpf(fmaf(c0, L0, c1 * L1) + fmaf(c2, L2, c3 * L3));
    const u32x4 keep = {~3u, ~3u, ~1u, ~1u};
    const f32x4 vb = lane < 32 ? __builtin_bit_cast(f32x4, xb & keep) : r;
    const f32x4 y = __builtin_bit_cast(f32x4, xa & keep) * (lane < 32 ? c1 : c2) + vb * (lane < 32 ? c3 : c0);
    const auto s0 = __builtin_amdgcn_permlane32_swap(__float_as_uint(y[0]), __float_as_uint(y[2]), false, false);
    const auto s1 = __builtin_amdgcn_permlane32_swap(__float_as_uint(y[1]), __float_as_uint(y[3]), false, false);
    __builtin_amdgcn_raw_buffer_store_b32(cvtpk((__uint_as_float(s0[0]) + __uint_as_float(s0[1])) * inv, (__uint_as_float(s1[0]) + __uint_as_float(s1[1])) * inv), rdst, oo, 0, 0);
    asm volatile("s_waitcnt vmcnt(0)" :: "{v[96:99]}"(xa0), "{v[100:103]}"(xb0), "{v[104:107]}"(xa1), "{v[108:111]}"(xb1) : "memory");
  }
}

extern "C" void kernel_launch(void* const* d_in, const int* in_sizes, int n_in, void* d_out, int out_size, void* d_ws, size_t ws_size, hipStream_t stream) {
  (void)hipFuncSetAttribute((const void*)gqa_decode, hipFuncAttributeMaxDynamicSharedMemorySize, LDS_BYTES);
  gqa_decode<<<NPAIR * GRP, (NW + 1) * 64, LDS_BYTES, stream>>>((const char*)d_in[1], (const char*)d_in[2], (const char*)d_in[0], (char*)d_ws, (unsigned*)d_out);
}
```
